```python
import torch

def _softmax(x, axis=-1):
    return torch.softmax(x, dim=axis)
def _gelu(x):
    return torch.nn.functional.gelu(x, approximate="tanh")
def _silu(x):
    return torch.nn.functional.silu(x)
def _sigmoid(x):
    return torch.sigmoid(x)
def _layernorm(x, axis=-1, eps=1e-5):
    m = x.mean(dim=axis, keepdim=True)
    v = x.var(dim=axis, keepdim=True, unbiased=False)
    return (x - m) / torch.sqrt(v + eps)
def _scatter_add(tgt, idx, src, axis=0):
    return tgt.clone().index_add_(axis, idx, src)
def _segment_sum(src, seg, n):
    out = torch.zeros((n,) + src.shape[1:], dtype=src.dtype)
    return out.index_add_(0, seg, src)
def _pad(x, pw):
    flat = tuple(v for pair in reversed(pw) for v in pair)
    return torch.nn.functional.pad(x, flat)

kernel_name = 'recurrent_scan__gqa_block_cached__glu_gated_bilinear__decode'

def setup_inputs():

    _w = (lambda a: (lambda t: t.to(torch.bfloat16) if t.is_floating_point() else t)(torch.as_tensor(a)))
    def _A_setup():
        B, Sq, D = 2, 1, 640
        Hq, Hkv, Sk, Dh = 32, 32, 4096, 192
        assert Hq % Hkv == 0, (Hq, Hkv)
        return {
            "x": _w((lambda *s: torch.randn(*s, dtype=torch.bfloat16) * 0.2)(B, Sq, D)),
            "k_cache": _w((lambda *s: torch.randn(*s, dtype=torch.bfloat16) * 1.6)(B, Hkv, Sk, Dh)),
            "v_cache": _w((lambda *s: torch.randn(*s, dtype=torch.bfloat16) * 1.6)(B, Hkv, Sk, Dh)),
            "wq": _w((lambda *s: torch.randn(*s, dtype=torch.bfloat16) * 1.6)(D, Hq * Dh) * (D ** -0.5)),
            "wo": _w((lambda *s: torch.randn(*s, dtype=torch.bfloat16) * 1.6)(Hq * Dh, D) * ((Hq * Dh) ** -0.5)),
        }
    _A_in = _A_setup()

    def _B_setup():
        B, S, D, F = 2, 1, 640, 1536
        return {
            "x": _w((lambda *s: torch.randn(*s, dtype=torch.bfloat16) * 0.2)(B, S, D)),
            "w_gate": _w((lambda *s: torch.randn(*s, dtype=torch.bfloat16) * 0.8)(D, F) * (D ** -0.5)),
            "w_up": _w((lambda *s: torch.randn(*s, dtype=torch.bfloat16) * 0.8)(D, F) * (D ** -0.5)),
            "w_down": _w((lambda *s: torch.randn(*s, dtype=torch.bfloat16) * 0.8)(F, D) * (F ** -0.5)),
        }
    _B_in = _B_setup()

    return {
        "A_x": _A_in["x"],
        "A_k_cache": _A_in["k_cache"],
        "A_v_cache": _A_in["v_cache"],
        "A_wq": _A_in["wq"],
        "A_wo": _A_in["wo"],
        "B_x": _B_in["x"],
        "B_w_gate": _B_in["w_gate"],
        "B_w_up": _B_in["w_up"],
        "B_w_down": _B_in["w_down"],
    }

def reference(A_x, A_k_cache, A_v_cache, A_wq, A_wo, B_x, B_w_gate, B_w_up, B_w_down):
    def _A_ref(x, k_cache, v_cache, wq, wo):
        B, Sq, D = x.shape
        Hq, Dh = 32, 192
        Hkv = k_cache.shape[1]
        rep = Hq // Hkv
        q = (lambda x, *d: x.permute(*d))((x @ wq).reshape(B, Sq, Hq, Dh), 0, 2, 1, 3)
        k = (lambda x, n, axis: torch.repeat_interleave(x, n, dim=axis))(k_cache, rep, axis=1)
        v = (lambda x, n, axis: torch.repeat_interleave(x, n, dim=axis))(v_cache, rep, axis=1)
        scores = torch.einsum("bhqd,bhkd->bhqk", q, k) * (Dh ** -0.5)
        p = _softmax(scores, axis=-1)
        o = torch.einsum("bhqk,bhkd->bhqd", p, v)
        return (lambda x, *d: x.permute(*d))(o, 0, 2, 1, 3).reshape(B, Sq, Hq * Dh) @ wo

    def _B_ref(x, w_gate, w_up, w_down):
        g = torch.einsum("bsd,df->bsf", x, w_gate)
        u = torch.einsum("bsd,df->bsf", x, w_up)
        h = g * u
        return torch.einsum("bsf,fd->bsd", h, w_down)

    x = A_x
    xn = _layernorm(x, axis=-1)
    s = _A_ref(xn, A_k_cache, A_v_cache, A_wq, A_wo)
    g = _silu(_B_ref(xn, B_w_gate, B_w_up, B_w_down))
    return x + s * g
```

```cpp
#include <hip/hip_runtime.h>
#include <hip/hip_bf16.h>
#include <stdint.h>

typedef unsigned v4u __attribute__((ext_vector_type(4)));
typedef float f32x4 __attribute__((ext_vector_type(4)));
typedef __bf16 bf16x2 __attribute__((ext_vector_type(2)));
#define GAS __attribute__((address_space(1)))

constexpr int NB = 2, D = 640, H = 32, SK = 4096, DH = 192, F = 1536;
constexpr int NSPLIT = 4;

__device__ __forceinline__ float bf2f(unsigned short b) { return __builtin_bit_cast(float, (unsigned)b << 16); }
__device__ __forceinline__ float rbf(float f) {
  return __builtin_bit_cast(float, (unsigned)__builtin_bit_cast(unsigned short, (__bf16)f) << 16);
}
__device__ __forceinline__ float lo16(unsigned u) { return __builtin_bit_cast(float, u << 16); }
__device__ __forceinline__ float hi16(unsigned u) { return __builtin_bit_cast(float, u & 0xffff0000u); }
__device__ __forceinline__ float dot2(unsigned k, unsigned q, float acc) {
  return __builtin_amdgcn_fdot2_f32_bf16(__builtin_bit_cast(bf16x2, k), __builtin_bit_cast(bf16x2, q), acc, false);
}
template <int CTRL> __device__ __forceinline__ float dpp(float x) {
  return __builtin_bit_cast(float, __builtin_amdgcn_mov_dpp(__builtin_bit_cast(int, x), CTRL, 0xf, 0xf, true));
}
constexpr int XOR1 = 0xB1, XOR2 = 0x4E, HMIR = 0x141, ROR8 = 0x128;

__device__ __forceinline__ float wave_sum(float v) {
  for (int o = 32; o; o >>= 1) v += __shfl_xor(v, o);
  return v;
}

__device__ void ln_to_lds(const unsigned short* x, float* xn  ) {
  const int tid = threadIdx.x, lane = tid & 63, w = tid >> 6;
  if (w < 2) {
    float v[10]; float s = 0.f;
    for (int i = 0; i < 10; ++i) { v[i] = bf2f(x[w * D + i * 64 + lane]); s += v[i]; }
    s = wave_sum(s);
    const float mean = s * (1.0f / D);
    float ss = 0.f;
    for (int i = 0; i < 10; ++i) { const float d = v[i] - mean; ss += d * d; }
    ss = wave_sum(ss);
    const float var = rbf(ss * (1.0f / D));
    const float mb = rbf(mean);
    const float sd = rbf(sqrtf(rbf(var + 1e-5f)));
    for (int i = 0; i < 10; ++i) xn[w * D + i * 64 + lane] = rbf(rbf(v[i] - mb) / sd);
  }
}

__global__ void __launch_bounds__(512) k_prep(const unsigned short* x, const unsigned short* wq, const unsigned short* wg, const unsigned short* wu,
                                              unsigned short* q, float* h) {
  __shared__ float xn[2 * D];
  __shared__ float red[2][64][2][64];
  ln_to_lds(x, xn);
  __syncthreads();
  const int tid = threadIdx.x, c = tid & 7, r = tid >> 3;
  const int blk = blockIdx.x;
  const bool isq = blk < 96;
  const int nmat = isq ? 1 : 2;
  for (int m = 0; m < nmat; ++m) {
    const unsigned short* W = isq ? wq : (m == 0 ? wg : wu);
    const int ld = isq ? 6144 : F;
    const int c0 = (isq ? blk : blk - 96) * 64 + c * 8;
    float a0[8] = {0}, a1[8] = {0};
    v4u t[10];
    for (int i = 0; i < 10; ++i) t[i] = __builtin_nontemporal_load((const v4u*)(W + (size_t)(r + 64 * i) * ld + c0));
    for (int i = 0; i < 10; ++i) {
      const float x0 = xn[r + 64 * i], x1 = xn[D + r + 64 * i];
      for (int k = 0; k < 4; ++k) {
        a0[2 * k] += x0 * lo16(t[i][k]); a0[2 * k + 1] += x0 * hi16(t[i][k]);
        a1[2 * k] += x1 * lo16(t[i][k]); a1[2 * k + 1] += x1 * hi16(t[i][k]);
      }
    }
    for (int k = 0; k < 8; ++k) { red[m][r][0][c * 8 + k] = a0[k]; red[m][r][1][c * 8 + k] = a1[k]; }
  }
  __syncthreads();
  if (tid < 128) {
    const int b = tid >> 6, col = tid & 63;
    float s0 = 0.f, s1 = 0.f;
    for (int i = 0; i < 64; ++i) { s0 += red[0][i][b][col]; if (!isq) s1 += red[1][i][b][col]; }
    if (isq) q[b * 6144 + blk * 64 + col] = __builtin_bit_cast(unsigned short, (__bf16)s0);
    else h[b * F + (blk - 96) * 64 + col] = rbf(rbf(s0) * rbf(s1));
  }
}

#define WIN 3
__global__ void __launch_bounds__(512, 2) k_attn(const unsigned short* kc, const unsigned short* vc, const unsigned short* q, float* part) {
  __shared__ float mrg[8][200];
  const int tid = threadIdx.x, lane = tid & 63, w = __builtin_amdgcn_readfirstlane(tid >> 6);
  const int wg = blockIdx.x;
  const int bh = wg >> 2, sp = wg & 3;
  const int r = lane >> 3, c = lane & 7;
  const size_t base = ((size_t)bh * SK + sp * 1024 + w * 128 + r) * DH + c * 8;
  const GAS v4u* kp = (const GAS v4u*)(kc + base);
  const GAS v4u* vp = (const GAS v4u*)(vc + base);
  v4u qf[3];
  {
    const int b = bh >> 5, hh = bh & 31;
    for (int i = 0; i < 3; ++i) qf[i] = *(const v4u*)(q + b * 6144 + hh * DH + (c + 8 * i) * 8);
  }
  v4u buf[WIN][3];
#define LD(j) do { _Pragma("unroll") for (int i_ = 0; i_ < 3; ++i_) buf[(j) % WIN][i_] = __builtin_nontemporal_load(((j) < 16 ? kp + (j) * 192 : vp + ((j) - 16) * 192) + i_ * 8); } while (0)
#pragma unroll
  for (int j = 0; j < WIN; ++j) LD(j);
  __builtin_amdgcn_sched_barrier(0);
  float s[16];
  const float SCALE = 0.07216878364870322f;
#pragma unroll
  for (int j = 0; j < 16; ++j) {
    float d0 = 0.f, d1 = 0.f, d2 = 0.f;
#pragma unroll
    for (int k = 0; k < 4; ++k) {
      d0 = dot2(buf[j % WIN][0][k], qf[0][k], d0);
      d1 = dot2(buf[j % WIN][1][k], qf[1][k], d1);
      d2 = dot2(buf[j % WIN][2][k], qf[2][k], d2);
    }
    float d = d0 + d1 + d2;
    if (j + WIN < 32) LD(j + WIN);
    d += dpp<XOR1>(d); d += dpp<XOR2>(d); d += dpp<HMIR>(d);
    s[j] = rbf(rbf(d) * SCALE);
    __builtin_amdgcn_sched_barrier(0);
  }
  float m = s[0];
#pragma unroll
  for (int j = 1; j < 16; ++j) m = fmaxf(m, s[j]);
  m = fmaxf(m, dpp<ROR8>(m));
  m = fmaxf(m, __shfl_xor(m, 16)); m = fmaxf(m, __shfl_xor(m, 32));
  float l = 0.f;
#pragma unroll
  for (int j = 0; j < 16; ++j) { s[j] = __builtin_amdgcn_exp2f((s[j] - m) * 1.4426950408889634f); l += s[j]; }
  l += dpp<ROR8>(l); l += __shfl_xor(l, 16); l += __shfl_xor(l, 32);
  float acc[24];
#pragma unroll
  for (int k = 0; k < 24; ++k) acc[k] = 0.f;
#pragma unroll
  for (int j = 16; j < 32; ++j) {
    const float p = s[j - 16];
#pragma unroll
    for (int i = 0; i < 3; ++i)
#pragma unroll
      for (int k = 0; k < 4; ++k) {
        acc[i * 8 + 2 * k] = fmaf(p, lo16(buf[j % WIN][i][k]), acc[i * 8 + 2 * k]);
        acc[i * 8 + 2 * k + 1] = fmaf(p, hi16(buf[j % WIN][i][k]), acc[i * 8 + 2 * k + 1]);
      }
    if (j + WIN < 32) LD(j + WIN);
    __builtin_amdgcn_sched_barrier(0);
  }
#pragma unroll
  for (int k = 0; k < 24; ++k) { float a = acc[k]; a += dpp<ROR8>(a); a += __shfl_xor(a, 16); a += __shfl_xor(a, 32); acc[k] = a; }
  if (r == 0) {
#pragma unroll
    for (int i = 0; i < 3; ++i)
#pragma unroll
      for (int k = 0; k < 8; ++k) mrg[w][(c + 8 * i) * 8 + k] = acc[i * 8 + k];
    if (c == 0) { mrg[w][192] = m; mrg[w][193] = l; }
  }
  __syncthreads();
  if (tid < 194) {
    float M = mrg[0][192];
    for (int i = 1; i < 8; ++i) M = fmaxf(M, mrg[i][192]);
    float o = 0.f;
    for (int i = 0; i < 8; ++i) o += __expf(mrg[i][192] - M) * mrg[i][tid == 192 ? 0 : tid];
    part[wg * 200 + tid] = tid == 192 ? M : o;
  }
}

__global__ void __launch_bounds__(256) k_wo(const float* part, const float* h, const unsigned short* wo, const unsigned short* wd, float* sp) {
  __shared__ float o[2][256];
  __shared__ float red[32][2][64];
  const int blk = blockIdx.x, tid = threadIdx.x;
  const bool isw = blk < 240;
  const int cg = blk % 10, rg = (isw ? blk : blk - 240) / 10;
  const unsigned short* W = isw ? wo : wd;
  const int c = tid & 7, r = tid >> 3;
  v4u t[8];
  for (int i = 0; i < 8; ++i) t[i] = __builtin_nontemporal_load((const v4u*)(W + (size_t)(rg * 256 + r + 32 * i) * D + cg * 64 + c * 8));
  for (int b = 0; b < 2; ++b) {
    const int row = rg * 256 + tid;
    if (isw) {
      const int hh = row / DH, d = row % DH;
      const float* pp = part + (size_t)((b * 32 + hh) * 4) * 200;
      float M = pp[192];
      for (int i = 1; i < 4; ++i) M = fmaxf(M, pp[i * 200 + 192]);
      float L = 0.f, a = 0.f;
      for (int i = 0; i < 4; ++i) { const float e = __expf(pp[i * 200 + 192] - M); L += e * pp[i * 200 + 193]; a += e * pp[i * 200 + d]; }
      o[b][tid] = rbf(a / L);
    } else o[b][tid] = h[b * F + row];
  }
  __syncthreads();
  float a0[8] = {0}, a1[8] = {0};
  for (int i = 0; i < 8; ++i) {
    const float x0 = o[0][r + 32 * i], x1 = o[1][r + 32 * i];
    for (int k = 0; k < 4; ++k) {
      a0[2 * k] += x0 * lo16(t[i][k]); a0[2 * k + 1] += x0 * hi16(t[i][k]);
      a1[2 * k] += x1 * lo16(t[i][k]); a1[2 * k + 1] += x1 * hi16(t[i][k]);
    }
  }
  for (int k = 0; k < 8; ++k) { red[r][0][c * 8 + k] = a0[k]; red[r][1][c * 8 + k] = a1[k]; }
  __syncthreads();
  if (tid < 128) {
    const int b = tid >> 6, col = tid & 63;
    float s = 0.f;
    for (int i = 0; i < 32; ++i) s += red[i][b][col];
    sp[(size_t)((isw ? rg : 24 + rg) * 2 + b) * D + cg * 64 + col] = s;
  }
}

__global__ void __launch_bounds__(64) k_fin(const float* sp, const unsigned short* x, unsigned short* out) {
  const int i = blockIdx.x * 64 + threadIdx.x;
  float s = 0.f, y = 0.f;
  for (int g = 0; g < 24; ++g) s += sp[g * 1280 + i];
  for (int g = 24; g < 30; ++g) y += sp[g * 1280 + i];
  s = rbf(s); y = rbf(y);
  const float g = rbf(y / (1.f + __expf(-y)));
  const float o = rbf(bf2f(x[i]) + rbf(s * g));
  out[i] = (unsigned short)(__builtin_bit_cast(unsigned, o) >> 16);
}

extern "C" void kernel_launch(void* const* d_in, const int* in_sizes, int n_in, void* d_out, int out_size, void* d_ws, size_t ws_size,
                              hipStream_t stream) {
  const unsigned short* x = (const unsigned short*)d_in[0];
  const unsigned short* kc = (const unsigned short*)d_in[1];
  const unsigned short* vc = (const unsigned short*)d_in[2];
  const unsigned short* wq = (const unsigned short*)d_in[3];
  const unsigned short* wo = (const unsigned short*)d_in[4];
  const unsigned short* wg = (const unsigned short*)d_in[6];
  const unsigned short* wu = (const unsigned short*)d_in[7];
  const unsigned short* wd = (const unsigned short*)d_in[8];
  char* ws = (char*)d_ws;
  unsigned short* q = (unsigned short*)ws;
  float* h = (float*)(ws + 32768);
  float* part = (float*)(ws + 65536);
  float* sp = (float*)(ws + 65536 + 262144);
  k_prep<<<120, 512, 0, stream>>>(x, wq, wg, wu, q, h);
  k_attn<<<256, 512, 0, stream>>>(kc, vc, q, part);
  k_wo<<<300, 256, 0, stream>>>(part, h, wo, wd, sp);
  k_fin<<<20, 64, 0, stream>>>(sp, x, (unsigned short*)d_out);
}
```

```cpp
#include <hip/hip_runtime.h>
#include <hip/hip_bf16.h>
#include <stdint.h>

typedef unsigned v4u __attribute__((ext_vector_type(4)));
typedef unsigned v2u __attribute__((ext_vector_type(2)));
typedef float f32x4 __attribute__((ext_vector_type(4)));
typedef float f32x2 __attribute__((ext_vector_type(2)));
typedef __bf16 bf16x2 __attribute__((ext_vector_type(2)));
typedef __bf16 bf16x8 __attribute__((ext_vector_type(8)));
#define LAS __attribute__((address_space(3)))
#define GAS __attribute__((address_space(1)))
typedef __amdgpu_buffer_rsrc_t rsrc_t;

constexpr int PFL = 6;
constexpr int DMIN = 5;
constexpr int DMAX = 12;
constexpr int GSH = 5;
constexpr int TBEG = 200;
constexpr int TEND = 2900;
constexpr int LAG = 6;
constexpr int NRND = 4;
constexpr int TS0 = 750;
constexpr int TSP = 470;
constexpr int AW1 = 90;
constexpr int AW2 = 300;
constexpr int ADELTA = 8;
constexpr int T0OFF = 30;
constexpr int QTIME = 530;
constexpr int WARM = 1500;
constexpr int YTIME = 2000;
constexpr int TPOLL = 220;
constexpr int MLDELAY = 68;
constexpr int SDELAY = 10;
constexpr int PSL = 6;
constexpr int FD = 5;
constexpr int ROTN = 13;
constexpr int GEND = 62;
constexpr int EBON = 3;
constexpr int VT = 61;
constexpr int FG = 60;

constexpr int DM = 640, SK = 4096, DH = 192, FF = 1536;
constexpr int AUX_NT = 2, AUX_SC1 = 16;
constexpr int OOB = (int)0x80000000;
constexpr int WS_EP = 0;
constexpr int WS_T0 = 128;
constexpr int WS_QX = 256;
constexpr int WS_ML = WS_QX + 72 * 3 * 64 * 16;
constexpr int WS_OP = WS_ML + 256 * 8 * 16;
constexpr int WS_SP = WS_OP + 2 * 256 * 48 * 16;
constexpr int WS_YP = WS_SP + 5 * 48 * 64 * 16;
constexpr int WS_PR = WS_YP + 5 * 24 * 64 * 16;
constexpr int WS_BC = WS_PR + NRND * 64 * 128;
constexpr int WS_END = WS_BC + NRND * 128;
constexpr unsigned SPIN_CAP = 1u << 18;
constexpr float LOG2E = 1.4426950408889634f;

__device__ __forceinline__ float asf(unsigned u) { return __builtin_bit_cast(float, u); }
__device__ __forceinline__ unsigned asu(float f) { return __builtin_bit_cast(unsigned, f); }
__device__ __forceinline__ unsigned pkbf(float lo, float hi) {
  bf16x2 v = {(__bf16)lo, (__bf16)hi};
  return __builtin_bit_cast(unsigned, v);
}
__device__ __forceinline__ float rbf(float f) { return asf(pkbf(f, f) << 16); }
__device__ __forceinline__ float lo16(unsigned u) { return asf(u << 16); }
__device__ __forceinline__ float hi16(unsigned u) { return asf(u & 0xffff0000u); }
__device__ __forceinline__ float dot2(unsigned k, unsigned q, float acc) {
  return __builtin_amdgcn_fdot2_f32_bf16(__builtin_bit_cast(bf16x2, k), __builtin_bit_cast(bf16x2, q), acc, false);
}
template <int CTRL> __device__ __forceinline__ float dpp(float x) { return asf(__builtin_amdgcn_mov_dpp(asu(x), CTRL, 0xf, 0xf, true)); }
template <int CTRL> __device__ __forceinline__ unsigned dppu(unsigned x) { return __builtin_amdgcn_mov_dpp(x, CTRL, 0xf, 0xf, true); }
constexpr int XOR1 = 0xB1, XOR2 = 0x4E, HMIR = 0x141, ROR4 = 0x124, ROR8 = 0x128;
__device__ __forceinline__ float sum8(float d) { d += dpp<XOR1>(d); d += dpp<XOR2>(d); d += dpp<HMIR>(d); return d; }
__device__ __forceinline__ float max8(float d) { d = fmaxf(d, dpp<XOR1>(d)); d = fmaxf(d, dpp<XOR2>(d)); return fmaxf(d, dpp<HMIR>(d)); }
__device__ __forceinline__ float sum_x16(float x) { auto s = __builtin_amdgcn_permlane16_swap(asu(x), asu(x), false, false); return asf(s[0]) + asf(s[1]); }
__device__ __forceinline__ float sum_x32(float x) { auto s = __builtin_amdgcn_permlane32_swap(asu(x), asu(x), false, false); return asf(s[0]) + asf(s[1]); }
__device__ __forceinline__ float max_x16(float x) { auto s = __builtin_amdgcn_permlane16_swap(asu(x), asu(x), false, false); return fmaxf(asf(s[0]), asf(s[1])); }
__device__ __forceinline__ float max_x32(float x) { auto s = __builtin_amdgcn_permlane32_swap(asu(x), asu(x), false, false); return fmaxf(asf(s[0]), asf(s[1])); }
__device__ __forceinline__ float fold4(const float (&r)[4]) {
  auto s0 = __builtin_amdgcn_permlane32_swap(asu(r[0]), asu(r[2]), false, false);
  auto s1 = __builtin_amdgcn_permlane32_swap(asu(r[1]), asu(r[3]), false, false);
  const float a = asf(s0[0]) + asf(s0[1]), b = asf(s1[0]) + asf(s1[1]);
  auto s2 = __builtin_amdgcn_permlane16_swap(asu(a), asu(b), false, false);
  float x = asf(s2[0]) + asf(s2[1]);
  x += dpp<ROR8>(x); x += dpp<ROR4>(x);
  return x;
}
__device__ __forceinline__ f32x4 mfma(v4u a, v4u b, f32x4 c) {
  return __builtin_amdgcn_mfma_f32_16x16x32_bf16(__builtin_bit_cast(bf16x8, a), __builtin_bit_cast(bf16x8, b), c, 0, 0, 0);
}
__device__ __forceinline__ v4u place(unsigned p, v4u sel) {
  return v4u{__builtin_amdgcn_perm(p, p, sel[0]), __builtin_amdgcn_perm(p, p, sel[1]), __builtin_amdgcn_perm(p, p, sel[2]), __builtin_amdgcn_perm(p, p, sel[3])};
}
__device__ __forceinline__ rsrc_t mkrs(const void* p, int bytes) { return __builtin_amdgcn_make_buffer_rsrc((void*)p, 0, bytes, 0x00020000); }
__device__ __forceinline__ unsigned clk() { return (unsigned)__builtin_amdgcn_s_memrealtime(); }

#define SB() __builtin_amdgcn_sched_barrier(0)
#define LDS_WAIT() asm volatile("s_waitcnt lgkmcnt(0)" ::: "memory")
#define BARRIER() do { LDS_WAIT(); __builtin_amdgcn_s_barrier(); asm volatile("" ::: "memory"); } while (0)
#define FLAG_SET(word, byte) (*((volatile LAS unsigned char*)&(word) + (byte)) = 1)
#define PAD(a, b) asm volatile("fk_pad" #a ":\n\ts_cbranch_execz fk_pad" #b)
#define PADS(a, b) "fk_pad" #a ":\n\ts_cbranch_execz fk_pad" #b "\n\t"

__device__ __forceinline__ void st_wide(rsrc_t rs, int off, unsigned a, unsigned b, unsigned tag) {
  __builtin_amdgcn_raw_buffer_store_b128(v4u{a, tag, b, tag}, rs, off, 0, AUX_SC1);
}
__device__ __forceinline__ void st_dense(rsrc_t rs, int off, f32x4 v, unsigned tag) {
  const unsigned ta = tag & 0x3ffu, tb = (tag >> 10) & 0x3ffu;
  __builtin_amdgcn_raw_buffer_store_b128(v4u{((asu(v[0]) + 0x200u) & ~0x3ffu) | ta, ((asu(v[1]) + 0x200u) & ~0x3ffu) | tb,
                                             ((asu(v[2]) + 0x200u) & ~0x3ffu) | ta, ((asu(v[3]) + 0x200u) & ~0x3ffu) | tb}, rs, off, 0, AUX_SC1);
}
__device__ __forceinline__ f32x4 dense_val(v4u g) { return f32x4{asf(g.x & ~0x3ffu), asf(g.y & ~0x3ffu), asf(g.z & ~0x3ffu), asf(g.w & ~0x3ffu)}; }
__device__ __forceinline__ unsigned dense_bad(v4u g, unsigned tag) { return ((g.x ^ tag) | (g.z ^ tag) | (g.y ^ (tag >> 10)) | (g.w ^ (tag >> 10))) & 0x3ffu; }
template <int SLP> __device__ __forceinline__ v4u gather_wide(rsrc_t rs, int off, unsigned tag) {
  v4u g;
  unsigned spins = 0;
#pragma nounroll
  for (;;) {
    asm volatile("" ::: "memory");
    g = __builtin_amdgcn_raw_buffer_load_b128(rs, off, 0, AUX_SC1);
    if (((g.y == tag) & (g.w == tag)) || ++spins >= SPIN_CAP) break;
    __builtin_amdgcn_s_sleep(SLP);
  }
  return g;
}
template <int N, int SLP> __device__ __forceinline__ void gather_dense(rsrc_t rs, const int (&off)[N], unsigned tag, v4u (&g)[N]) {
  unsigned spins = 0;
#pragma nounroll
  for (;;) {
    asm volatile("" ::: "memory");
    unsigned bad = 0u;
#pragma unroll
    for (int k = 0; k < N; ++k) g[k] = __builtin_amdgcn_raw_buffer_load_b128(rs, off[k], 0, AUX_SC1);
#pragma unroll
    for (int k = 0; k < N; ++k) bad |= dense_bad(g[k], tag);
    if (bad == 0u || ++spins >= SPIN_CAP) break;
    __builtin_amdgcn_s_sleep(SLP);
  }
}
#define P_LD(R, SO) "buffer_load_dwordx4 " R ", %[off], %[rs], " SO " offen sc1\n\t"
#define P_BAD(T, X, Y, Z, W) "v_bitop3_b32 " T ", " X ", " Z ", %[ta] bitop3:0x7e\n\tv_bitop3_b32 %[t3], " Y ", " W ", %[tb] bitop3:0x7e\n\tv_or_b32 " T ", " T ", %[t3]\n\t"
#define P_NEXT "s_andn2_b64 %[pend], %[pend], exec\n\ts_cbranch_scc0 2f\n\ts_mov_b64 exec, %[pend]\n\t"
#define P1_TAKE(X, Y, Z, W) P_BAD("%[t0]", X, Y, Z, W) "v_and_b32 %[t0], 0x3ff, %[t0]\n\tv_cmpx_eq_u32 vcc, 0, %[t0]\n\t" \
  "v_and_b32 %[r0], %[mk], " X "\n\tv_and_b32 %[r1], %[mk], " Y "\n\tv_and_b32 %[r2], %[mk], " Z "\n\tv_and_b32 %[r3], %[mk], " W "\n\t"
__device__ __forceinline__ void poll1(rsrc_t rs, int off, unsigned tag, float (&r)[4], v4u& a, v4u& b) {
  unsigned long long pend; unsigned cnt, t0, t3;
  asm volatile(
      "s_mov_b64 %[pend], exec\n\ts_mov_b32 %[cnt], 0x40000\n"
      "1:\n\t"
      "s_waitcnt vmcnt(1)\n\t" P1_TAKE("v232", "v233", "v234", "v235") P_NEXT
      P_LD("v[232:235]", "0") "s_waitcnt vmcnt(1)\n\t" P1_TAKE("v236", "v237", "v238", "v239") P_NEXT
      P_LD("v[236:239]", "0")
      "s_sub_u32 %[cnt], %[cnt], 1\n\ts_cbranch_scc0 1b\n"
      "2:\n\ts_mov_b64 exec, -1\n\t"
      : [pend] "=&s"(pend), [cnt] "=&s"(cnt), [t0] "=&v"(t0), [t3] "=&v"(t3), [r0] "=&v"(r[0]), [r1] "=&v"(r[1]), [r2] "=&v"(r[2]), [r3] "=&v"(r[3]),
        "+{v[232:235]}"(a), "+{v[236:239]}"(b)
      : [off] "v"(off), [rs] "s"(rs), [ta] "v"(tag), [tb] "v"(tag >> 10), [mk] "s"(~0x3ffu)
      : "vcc", "scc", "memory");
}
#define P3_SUM(R, X, Y, Z) "v_and_b32 " R ", %[mk], " X "\n\tv_and_b32 %[t0], %[mk], " Y "\n\tv_and_b32 %[t1], %[mk], " Z "\n\t" \
  "v_add_f32 " R ", " R ", %[t0]\n\tv_add_f32 " R ", " R ", %[t1]\n\t"
#define P3_TAKE(A0, A1, A2, A3, B0, B1, B2, B3, C0, C1, C2, C3) \
  P_BAD("%[t0]", A0, A1, A2, A3) P_BAD("%[t1]", B0, B1, B2, B3) P_BAD("%[t2]", C0, C1, C2, C3) \
  "v_or3_b32 %[t0], %[t0], %[t1], %[t2]\n\tv_and_b32 %[t0], 0x3ff, %[t0]\n\tv_cmpx_eq_u32 vcc, 0, %[t0]\n\t" \
  P3_SUM("%[r0]", A0, B0, C0) P3_SUM("%[r1]", A1, B1, C1) P3_SUM("%[r2]", A2, B2, C2) P3_SUM("%[r3]", A3, B3, C3)
__device__ __forceinline__ void poll3(rsrc_t rs, int off, int so1, int so2, unsigned tag, float (&r)[4], v4u (&a)[3], v4u (&b)[3]) {
  unsigned long long pend; unsigned cnt, t0, t1, t2, t3;
  asm volatile(
      "s_mov_b64 %[pend], exec\n\ts_mov_b32 %[cnt], 0x40000\n\t"
      P_LD("v[208:211]", "0") P_LD("v[212:215]", "%[so1]") P_LD("v[216:219]", "%[so2]") "s_sleep %[slp]\n"
      "1:\n\t"
      P_LD("v[220:223]", "0") P_LD("v[224:227]", "%[so1]") P_LD("v[228:231]", "%[so2]") PADS(16, 17) "s_waitcnt vmcnt(3)\n\t"
      P3_TAKE("v208", "v209", "v210", "v211", "v212", "v213", "v214", "v215", "v216", "v217", "v218", "v219") P_NEXT
      P_LD("v[208:211]", "0") P_LD("v[212:215]", "%[so1]") P_LD("v[216:219]", "%[so2]") PADS(17, 18) "s_waitcnt vmcnt(3)\n\t"
      P3_TAKE("v220", "v221", "v222", "v223", "v224", "v225", "v226", "v227", "v228", "v229", "v230", "v231") P_NEXT
      "s_sub_u32 %[cnt], %[cnt], 1\n\ts_cbranch_scc0 1b\n"
      "2:\n\ts_mov_b64 exec, -1\n\ts_nop 1\n\t"
      : [pend] "=&s"(pend), [cnt] "=&s"(cnt), [t0] "=&v"(t0), [t1] "=&v"(t1), [t2] "=&v"(t2), [t3] "=&v"(t3),
        [r0] "=&v"(r[0]), [r1] "=&v"(r[1]), [r2] "=&v"(r[2]), [r3] "=&v"(r[3]),
        "=&{v[208:211]}"(a[0]), "=&{v[212:215]}"(a[1]), "=&{v[216:219]}"(a[2]), "=&{v[220:223]}"(b[0]), "=&{v[224:227]}"(b[1]), "=&{v[228:231]}"(b[2])
      : [off] "v"(off), [rs] "s"(rs), [so1] "s"(so1), [so2] "s"(so2), [ta] "v"(tag), [tb] "v"(tag >> 10), [mk] "s"(~0x3ffu), [slp] "i"(PSL)
      : "vcc", "scc", "memory");
}
template <int SLP> __device__ __forceinline__ void lds_wait_ge(volatile LAS unsigned* p, unsigned v) {
#pragma nounroll
  for (unsigned spins = 0; spins < (SPIN_CAP << 5) / SLP; ++spins) {
    if (__builtin_amdgcn_readfirstlane(*p) >= v) break;
    __builtin_amdgcn_s_sleep(SLP);
  }
  asm volatile("" ::: "memory");
}
template <int SLP> __device__ __forceinline__ void wait_until(unsigned due) {
#pragma nounroll
  for (int i = 0; i < 1536 / SLP && (int)(clk() - due) < 0; ++i) __builtin_amdgcn_s_sleep(SLP);
}

struct Lds {
  float part[2][8][DH];
  f32x4 red[2][4][64];
  unsigned xn[4][DM];
  unsigned q[2][96];
  f32x2 ml[2][4];
  unsigned short hx[4][128];
  float fac[2][4];
  unsigned qready, vtrig, gcnt, facok;
  unsigned kdone, pdone, epoch, epok;
  unsigned eo[4];
  unsigned tref, tslope;
};


__global__ void __launch_bounds__(512, 2)
fused_kernel(const unsigned char* __restrict__ x, const unsigned char* __restrict__ kc, const unsigned char* __restrict__ vc,
             const unsigned char* __restrict__ wq, const unsigned char* __restrict__ wo, const unsigned char* __restrict__ wgt,
             const unsigned char* __restrict__ wup, const unsigned char* __restrict__ wdn, unsigned char* __restrict__ out, unsigned char* ws) {
  __shared__ __attribute__((aligned(16))) Lds L;
  const int tid = threadIdx.x, lane = tid & 63, wave = __builtin_amdgcn_readfirstlane(tid >> 6);
  const int wg = blockIdx.x, h = wg >> 3, j = wg & 7;
  const int du = ((j - h) & 7) * 32 + h;
  const bool isQ = du < 216;
  const int kvo = (h * 128 + j) * 12288, rot = (ROTN * h) >> 2;
  const int voff = (wave & 3) * 3072 + (lane >> 4) * 768 + (lane & 15) * 16;
#define UNIT(r_, n_) (kvo + (r_) * (32 * 128 * 12288) + (((n_) + rot) & 15) * (8 * 12288))
  const int u0 = UNIT(0, 0), u1 = UNIT(0, 1);
  const unsigned long long t64 = __builtin_amdgcn_s_memrealtime();
  const unsigned t0 = (unsigned)t64;
  const int hw = wave & 3, g4 = lane >> 4, col = lane & 15, lb = (lane >> 3) & 1, jj = lane & 7;
  const int qt = du / 3, qr = du % 3, stride = qt < 48 ? 12288 : 3072;
  const int vq = (qr * 208 + hw * 4 + g4) * stride + (qt < 48 ? qt : qt < 60 ? qt - 48 : qt - 60) * 256 + col * 16;
  const int os = lb * 1280 + (jj + 8 * g4) * 40;
  asm volatile("" ::"s"(u0), "s"(u1), "v"(voff), "v"(vq), "v"(os));
  asm volatile("" ::"s"(x), "s"(kc), "s"(vc), "s"(wq), "s"(wo), "s"(wgt), "s"(wup), "s"(wdn), "s"(out), "s"(ws), "s"(t64));
  const rsrc_t rws = mkrs(ws, WS_END);

  const rsrc_t rk = mkrs(kc, 2 * 32 * SK * DH * 2), rv = mkrs(vc, 2 * 32 * SK * DH * 2);
  const rsrc_t rx = mkrs(x, 2 * DM * 2);
  v4u buf[64][3];
  v4u wt[14], xs0, xs1; v2u xs2;
  unsigned ep;
  if (wave >= 4) {
#pragma unroll
    for (int i = 0; i < 6; ++i) { buf[i / 3][i % 3] = __builtin_amdgcn_raw_buffer_load_b128(rk, voff + (i % 3) * 256, i < 3 ? u0 : u1, AUX_NT); SB(); }
    *(LAS v4u*)&L.qready = v4u{0u, 0u, 0u, 0u};
    *(LAS v4u*)&L.kdone = v4u{0u, 0u, 0u, 0u};
    *(LAS v4u*)&L.eo[0] = v4u{t0, t0, t0, t0};
  } else if (isQ) {
    ep = __builtin_amdgcn_raw_buffer_load_b32(rws, wave == 2 ? WS_EP : OOB, 0, AUX_SC1);
    xs0 = __builtin_amdgcn_raw_buffer_load_b128(rx, os, 0, 0); xs1 = __builtin_amdgcn_raw_buffer_load_b128(rx, os + 16, 0, 0);
    xs2 = __builtin_amdgcn_raw_buffer_load_b64(rx, os + 32, 0, 0);
    const rsrc_t rw = mkrs(qt < 48 ? wq : qt < 60 ? wgt : wup, DM * stride);
#pragma unroll
    for (int t = 0; t < 14; ++t) { wt[t] = __builtin_amdgcn_raw_buffer_load_b128(rw, (t < 13 || qr == 2) ? vq : OOB, t * 16 * stride, AUX_NT); SB(); }
    asm volatile("" ::: "memory");
    SB();
  }
  BARRIER();
  SB();
  int wave2 = wave;
  asm volatile("" : "+s"(wave2));

  if (__builtin_expect(wave2 >= 4, 1)) {
    const int w = hw;
    unsigned long long wtab;
    asm volatile("s_getpc_b64 %0\n\ts_branch 1f\n\t"
                 ".irp k,0,1,2,3,4,5,6,7,8,9,10,11,12,13,14,15\n\ts_waitcnt vmcnt(\\k)\n\ts_setpc_b64 s[98:99]\n\t.endr\n1:" : "=s"(wtab));
    wtab -= 4;
    static_assert(DMIN >= 1 && DMAX <= 16 && DMAX <= 3 * LAG, "wait table");
    unsigned ts = t0; int tb = 0;
#define ISSUE(g_) do { \
      const unsigned ev_ = now - (TBEG + (g_) * (TEND - TBEG) / 63); \
      const int lag_ = (int)(ev_ - ts) - ((g_) >= 16 ? (tb * (g_)) >> 8 : 0); \
      const unsigned long long tgt_ = wtab + 8 * min(min(max(PFL + (lag_ >> GSH), DMIN), DMAX) + ((g_) >= GEND ? EBON * ((g_) - GEND + 1) : 0), 16); \
      const int so2_ = UNIT((g_) >> 5, (g_) & 15); \
      if ((g_) >= 6 && (g_) < 58 && ((g_) & 1) == 0) *(volatile LAS unsigned*)&L.eo[w] = ev_; \
      _Pragma("unroll") for (int i_ = 0; i_ < 3; ++i_) { \
        asm volatile("s_swappc_b64 s[98:99], %0" ::"s"(tgt_) : "s98", "s99"); SB(); \
        buf[g_][i_] = __builtin_amdgcn_raw_buffer_load_b128(((g_) & 16) ? rv : rk, voff + i_ * 256, so2_, AUX_NT); SB(); } } while (0)
#define NEXT_OF(g_) ((g_) < 2 * (16 - LAG) ? (((g_) & 1) ? -1 : 16 + (g_) / 2) : ((g_) + LAG < 64 ? (g_) + LAG : -1))
#define STEP_BEGIN() const int nx = NEXT_OF(g); const unsigned now = nx >= 0 ? clk() : 0u; \
      const v2u tsv = (nx > 16 && (nx & 1)) ? *(volatile LAS v2u*)&L.tref : v2u{0u, 0u}; SB()
#define STEP_END() do { SB(); if (nx > 16 && (nx & 1)) { ts = __builtin_amdgcn_readfirstlane(tsv.x); tb = __builtin_amdgcn_readfirstlane(tsv.y); } \
      if (nx >= 0) ISSUE(nx >= 0 ? nx : 0); } while (0)
#pragma unroll
    for (int n = 2; n < 16; ++n) { const unsigned now = clk(); SB(); ISSUE(n);
    }
    v4u msk;
#pragma unroll
    for (int e = 0; e < 4; ++e) msk[e] = ((jj >> 1) == e) ? ((jj & 1) ? 0xffff0000u : 0x0000ffffu) : 0u;
    lds_wait_ge<1>((volatile LAS unsigned*)&L.qready, 0x0100u);
    ts = __builtin_amdgcn_readfirstlane(*(volatile LAS unsigned*)&L.tref);
    unsigned tag = *(volatile LAS unsigned*)&L.epoch + 1u;
    int mpo = (lane & 3) == 0 ? WS_OP + ((256 + wg) * 48 + w * 16 + (lane >> 2)) * 16 : OOB;
    const LAS float* mp = (const LAS float*)&L.part[1][(lane & 3) * 2][4 * ((w * 16 + (lane >> 2)) % 48)];
#pragma unroll
    for (int r = 0; r < 2; ++r) {
      if (r == 1) lds_wait_ge<1>((volatile LAS unsigned*)&L.qready, 0x0101u);
      v4u qf[3];
#pragma unroll
      for (int i = 0; i < 3; ++i) qf[i] = *(const LAS v4u*)&L.q[r][4 * ((16 * i + col) % 24)];
      float s[16];
#pragma unroll
      for (int n = 0; n < 16; ++n) {
        const int g = 32 * r + n;
        STEP_BEGIN();
        float d0 = 0.f, d1 = 0.f, d2 = 0.f;
#pragma unroll
        for (int k = 0; k < 4; ++k) {
          d0 = dot2(buf[g][0][k], qf[0][k], d0); d1 = dot2(buf[g][1][k], qf[1][k], d1); d2 = dot2(buf[g][2][k], qf[2][k], d2);
        }
        const float dk = ((lb ? d2 : d0) + d1) + dpp<ROR8>(lb ? d0 : d2);
        s[n] = rbf(rbf(sum8(dk)) * 0.07216878364870322f);
        asm volatile("" : "+v"(s[n]));
        STEP_END();
      }
      float m = s[0];
#pragma unroll
      for (int n = 1; n < 16; ++n) m = fmaxf(m, s[n]);
      m = fmaxf(m, dpp<ROR8>(m)); m = max_x32(max_x16(m));
      float l = 0.f;
#pragma unroll
      for (int n = 0; n < 16; ++n) { const float p = __builtin_amdgcn_exp2f((s[n] - m) * LOG2E); l += p; s[n] = asf(pkbf(p, p)); }
      l += dpp<ROR8>(l); l = sum_x32(sum_x16(l));
      *(LAS f32x2*)&L.ml[r][w] = f32x2{m, l};
      if (r == 1) { LDS_WAIT(); FLAG_SET(L.kdone, w); }
      f32x4 acc[3];
#pragma unroll
      for (int i = 0; i < 3; ++i) acc[i] = f32x4{0.f, 0.f, 0.f, 0.f};
      float fac = 0.f;
#pragma unroll
      for (int n = 0; n < 16; ++n) {
        const int g = 32 * r + 16 + n;
        STEP_BEGIN();
        const unsigned pp = asu(s[n]);
        const v4u a = {pp & msk[0], pp & msk[1], pp & msk[2], pp & msk[3]};
#pragma unroll
        for (int i = 0; i < 3; ++i) acc[i] = mfma(a, buf[g][i], acc[i]);
        asm volatile("" : "+v"(acc[0]), "+v"(acc[1]), "+v"(acc[2]));
        STEP_END();
        if (g == VT) FLAG_SET(L.vtrig, w);
        if (g == 54) PAD(0, 1);
        if (g == 57) PAD(1, 2);
        if (g == 59) PAD(2, 3);
        if (g == 61) PAD(3, 4);
        if (g == 63) PAD(4, 5);
        if (g == FG) {
          lds_wait_ge<1>((volatile LAS unsigned*)&L.facok, 1u);
          fac = *(volatile LAS float*)&L.fac[1][w];
          asm volatile("" : "+v"(mpo), "+v"(mp), "+v"(fac), "+v"(tag));
          SB();
        }
      }
      {
        LAS float* pp_ = (LAS float*)&L.part[r][w * 2 + (lane >> 5)][4 * (g4 & 1)];
        const f32x4 va = lane < 32 ? acc[0] : acc[2];
        *(LAS f32x4*)(pp_ + 8 * (col + (lane < 32 ? 0 : 8))) = r == 0 ? va : va * fac;
        if ((lane >> 5) == lb) *(LAS f32x4*)(pp_ + 8 * (jj + (lane < 32 ? 16 : 0))) = r == 0 ? acc[1] : acc[1] * fac;
      }
      if (r == 1) { asm volatile("" ::: "memory"); FLAG_SET(L.pdone, w); }
    }
    PAD(5, 6);
    if (w < 3) {
      f32x4 oa, ob;
#pragma nounroll
      for (unsigned spins = 0; spins < (SPIN_CAP << 6); ++spins) {
        const unsigned f = *(volatile LAS unsigned*)&L.pdone;
        oa = *(const volatile LAS f32x4*)mp; ob = *(const volatile LAS f32x4*)(mp + DH);
        if (__builtin_amdgcn_readfirstlane(f) == 0x01010101u) break;
      }
      PAD(6, 7);
      f32x4 o = oa + ob;
#pragma unroll
      for (int e = 0; e < 4; ++e) { o[e] += dpp<XOR1>(o[e]); o[e] += dpp<XOR2>(o[e]); }
      st_dense(rws, mpo, o, tag);
    }
    return;
  }

  int pwo = OOB;
  if (hw == 3) {
    if (lane < 12) pwo = WS_OP + ((lane / 6) * 256 + wg) * 768 + (lane % 6) * 128;
    else if (lane == 12) pwo = WS_ML + wg * 128;
    else if (lane >= 32 && lane < 40) { if (du < 240) pwo = WS_SP + du * 1024 + (lane - 32) * 128; }
    else if (lane >= 48 && lane < 56) { if (du < 120) pwo = WS_YP + du * 1024 + (lane - 48) * 128; }
  }
  const unsigned pw0 = __builtin_amdgcn_raw_buffer_load_b32(rws, pwo, 0, AUX_SC1);
  SB();
  v4u selE, selO;
#pragma unroll
  for (int e = 0; e < 4; ++e) {
    selE[e] = ((jj >> 1) == e) ? ((jj & 1) ? 0x01000c0cu : 0x0c0c0100u) : 0x0c0c0c0cu;
    selO[e] = ((jj >> 1) == e) ? ((jj & 1) ? 0x03020c0cu : 0x0c0c0302u) : 0x0c0c0c0cu;
  }
  const int gpos = (g4 >> 1) * 32 + col * 2 + (g4 & 1);
  if (isQ) {
    if (hw == 2) { *(volatile LAS unsigned*)&L.epoch = ep; *(volatile LAS unsigned*)&L.epok = 1u; }
    float xv[20];
#pragma unroll
    for (int k = 0; k < 4; ++k) { xv[2 * k] = lo16(xs0[k]); xv[2 * k + 1] = hi16(xs0[k]); xv[8 + 2 * k] = lo16(xs1[k]); xv[9 + 2 * k] = hi16(xs1[k]); }
    xv[16] = lo16(xs2[0]); xv[17] = hi16(xs2[0]); xv[18] = lo16(xs2[1]); xv[19] = hi16(xs2[1]);
    float sm = 0.f;
#pragma unroll
    for (int k = 0; k < 20; ++k) sm += xv[k];
    sm = sum_x32(sum_x16(sum8(sm)));
    const float mean = sm * (1.0f / DM);
    float sq = 0.f;
#pragma unroll
    for (int k = 0; k < 20; ++k) { const float dv = xv[k] - mean; sq = fmaf(dv, dv, sq); }
    sq = sum_x32(sum_x16(sum8(sq)));
    const float mb = rbf(mean);
    const float sd = rbf(sqrtf(rbf(rbf(sq * (1.0f / DM)) + 1e-5f)));
    const float rsd = 1.0f / sd;
    unsigned np[10];
#pragma unroll
    for (int k = 0; k < 10; ++k) {
      const unsigned dp = pkbf(xv[2 * k] - mb, xv[2 * k + 1] - mb);
      np[k] = pkbf(lo16(dp) * rsd, hi16(dp) * rsd);
    }
#pragma unroll
    for (int k = 0; k < 5; ++k) *(LAS v2u*)&L.xn[hw][lb * 320 + (jj + 8 * g4) * 10 + 2 * k] = v2u{np[2 * k], np[2 * k + 1]};
    LDS_WAIT();
    const LAS unsigned short* xp = (const LAS unsigned short*)&L.xn[hw][lb * 320] + qr * 208 + hw * 4 + g4;
    unsigned xa[14];
#pragma unroll
    for (int t = 0; t < 14; ++t) xa[t] = xp[16 * t];
    f32x4 a0 = {0.f, 0.f, 0.f, 0.f}, a1 = {0.f, 0.f, 0.f, 0.f};
#pragma unroll
    for (int t = 0; t < 14; t += 2) {
      a0 = mfma(place(xa[t], selE), wt[t], a0);
      a1 = mfma(place(xa[t + 1], selE), wt[t + 1], a1);
    }
    *(LAS f32x4*)&L.red[0][hw][lane] = a0 + a1;
    LDS_WAIT();
    FLAG_SET(L.gcnt, hw);
    if (hw == 0) {
      lds_wait_ge<1>((volatile LAS unsigned*)&L.gcnt, 0x01010101u);
      st_dense(rws, WS_QX + (du * 64 + gpos) * 16, (L.red[0][0][lane] + L.red[0][1][lane]) + (L.red[0][2][lane] + L.red[0][3][lane]), *(volatile LAS unsigned*)&L.epoch + 1u);
    }
  } else if (hw == 2) {
    *(volatile LAS unsigned*)&L.epoch = __builtin_amdgcn_raw_buffer_load_b32(rws, WS_EP, 0, AUX_SC1);
    *(volatile LAS unsigned*)&L.epok = 1u;
  }
  lds_wait_ge<2>((volatile LAS unsigned*)&L.epok, 1u);
  const unsigned tag = *(volatile LAS unsigned*)&L.epoch + 1u;
  asm volatile("" ::"v"(pw0));
  if (wg == 0 && hw == 3) {
    const unsigned ta = tag & 0x3ffu, tb = (tag >> 10) & 0x3ffu;
    __builtin_amdgcn_raw_buffer_store_b128(v4u{(t0 << 10) | ta, (t0 & ~0x3ffu) | tb, ta, tb}, rws, lane < 8 ? WS_T0 + lane * 16 : OOB, 0, AUX_SC1);
  }
  wait_until<4>(t0 + QTIME);
  if (hw >= 2) {
    const int qb = hw - 2, gq = h * 48 + (lane < 48 ? lane : 0);
    const bool tl = qb == 0 && lane == 63;
    const int o0 = WS_QX + ((gq >> 5) * 192 + qb * 32 + (gq & 31)) * 16;
    v4u g[3]; const int off[3] = {tl ? WS_T0 : o0, o0 + 1024, o0 + 2048};
    gather_dense<3, 4>(rws, off, tag, g);
    if (tl) *(LAS v2u*)&L.tref = v2u{((g[0].x >> 10) | (g[0].y & ~0x3ffu)) + T0OFF, 0u};
    const f32x4 q4 = (dense_val(g[0]) + dense_val(g[1])) + dense_val(g[2]);
    if (lane < 48) *(LAS v2u*)&L.q[qb][2 * lane] = v2u{pkbf(q4[0], q4[1]), pkbf(q4[2], q4[3])};
    LDS_WAIT();
    FLAG_SET(L.qready, 1 - qb);
  }
  if (!isQ && hw != 0) {
    const int fu = (du - 216) * 3 + hw - 1, ru = fu / 5, ct = fu % 5;
    const rsrc_t rd = mkrs(wdn, FF * DM * 2);
    const int vd = (ru * 64 + g4 * 16) * 1280 + ct * 256 + col * 16;
    v4u wd[16];
#pragma unroll
    for (int t = 0; t < 16; ++t) {
      if (t >= FD) asm volatile("s_waitcnt vmcnt(%0)" ::"i"(FD - 1));
      SB();
      wd[t] = __builtin_amdgcn_raw_buffer_load_b128(rd, vd + (t & 1) * 1280, (t >> 1) * 2560, AUX_NT);
      SB();
    }
    const int go = WS_QX + ((48 + (lane >> 5) * 12 + (ru >> 1)) * 192 + (g4 & 1) * 32 + (ru & 1) * 16 + col) * 16;
    v4u g[3]; const int off[3] = {go, go + 1024, go + 2048};
    gather_dense<3, 16>(rws, off, tag, g);
    const f32x4 gu = (dense_val(g[0]) + dense_val(g[1])) + dense_val(g[2]);
    float hv[4];
#pragma unroll
    for (int e = 0; e < 4; ++e) { const float a = rbf(gu[e]); auto sw = __builtin_amdgcn_permlane32_swap(asu(a), asu(a), false, false); hv[e] = asf(sw[0]) * asf(sw[1]); }
    *(LAS v2u*)&L.hx[hw][4 * (lane & 31)] = v2u{pkbf(hv[0], hv[1]), pkbf(hv[2], hv[3])};
    LDS_WAIT();
    const v4u Pa = *(const volatile LAS v4u*)&L.hx[hw][lb * 64 + g4 * 16], Pb = *(const volatile LAS v4u*)&L.hx[hw][lb * 64 + g4 * 16 + 8];
    f32x4 y0 = {0.f, 0.f, 0.f, 0.f}, y1 = {0.f, 0.f, 0.f, 0.f};
#pragma unroll
    for (int k = 0; k < 8; ++k) {
      const unsigned pk = k < 4 ? Pa[k & 3] : Pb[k & 3];
      y0 = mfma(place(pk, selE), wd[2 * k], y0); y1 = mfma(place(pk, selO), wd[2 * k + 1], y1);
    }
    st_dense(rws, WS_YP + ((ct * 24 + ru) * 64 + gpos) * 16, y0 + y1, tag);
  }
  if (hw == 0) {
    lds_wait_ge<8>((volatile LAS unsigned*)&L.qready, 0x0100u);
    const unsigned tr0 = __builtin_amdgcn_readfirstlane(*(volatile LAS unsigned*)&L.tref);
    float ep_ = 0.f, gp = 0.f, bs = 0.f;
#pragma nounroll
    for (int k = 0; k < NRND; ++k) {
      const unsigned tk = tr0 + TS0 + k * TSP;
      if ((h & 3) == 0) {
        wait_until<2>(tk);
        const v4u e4 = *(volatile LAS v4u*)&L.eo[0];
        const unsigned es = (e4.x - tr0) + (e4.y - tr0) + (e4.z - tr0) + (e4.w - tr0);
        st_wide(rws, lane < 8 ? WS_PR + ((k * 64 + (h >> 2) * 8 + j) * 8 + lane) * 16 : OOB, es, es, tag);
      }
      wait_until<4>(tk + (du == 255 ? AW1 : AW2));
      v4u g;
      if (du == 255) {
        g = gather_wide<2>(rws, WS_PR + (k * 64 + lane) * 128, tag);
        float e = sum8((float)(int)g.x);
        e = sum_x32(sum_x16(e + dpp<ROR8>(e))) * (1.0f / 256);
        const float gk = ((float)(TS0 + k * TSP - TBEG) - e) * (63.0f / (TEND - TBEG)) - 1.0f;
        if (k > 0) bs = 0.5f * (bs + (e - ep_) / fmaxf(gk - gp, 1.0f));
        ep_ = e; gp = gk;
        g.x = tr0 + (int)(e - bs * gk) - ADELTA;
        g.z = (unsigned)(int)(bs * 256.0f);
        st_wide(rws, lane < 8 ? WS_BC + k * 128 + lane * 16 : OOB, g.x, g.z, tag);
      } else g = gather_wide<8>(rws, WS_BC + k * 128, tag);
      *(volatile LAS v2u*)&L.tref = v2u{g.x, g.z};
    }
  }
  if (hw == 2) {
    wait_until<16>(t0 + WARM);
    unsigned long long sv;
    asm volatile("s_mov_b64 %0, exec\n\ts_mov_b64 exec, 0\n\ts_branch fk_pad0\nfk_padback:\n\ts_mov_b64 exec, %0" : "=&s"(sv));
  }
  if (hw == 3) {
    lds_wait_ge<8>((volatile LAS unsigned*)&L.kdone, 0x01010101u);
    const f32x2 mine = *((const LAS f32x2*)&L.ml[0][0] + (lane & 7));
    st_wide(rws, lane < 8 ? WS_ML + (wg * 8 + lane) * 16 : OOB, asu(mine.x), asu(mine.y), tag);
    __builtin_amdgcn_s_sleep(MLDELAY);
    const v4u g = gather_wide<8>(rws, WS_ML + ((h * 8 + ((lane >> 2) & 7)) * 8 + (lane >> 5) * 4 + (lane & 3)) * 16, tag);
    const float m = asf(g.x);
    float M = max8(m); M = max_x16(fmaxf(M, dpp<ROR8>(M)));
    const float e = __builtin_amdgcn_exp2f((m - M) * LOG2E);
    float T = sum8(e * asf(g.z)); T = sum_x16(T + dpp<ROR8>(T));
    if (((lane >> 2) & 7) == j) L.fac[lane >> 5][lane & 3] = e / T;
    LDS_WAIT();
    *(volatile LAS unsigned*)&L.facok = 1u;
    const f32x4 f0 = *(const volatile LAS f32x4*)&L.fac[0][0];
    const int gq = lane < 48 ? lane : 0;
    f32x4 o0 = {0.f, 0.f, 0.f, 0.f};
#pragma unroll
    for (int u = 0; u < 4; ++u) o0 += (*(const LAS f32x4*)&L.part[0][2 * u][4 * gq] + *(const LAS f32x4*)&L.part[0][2 * u + 1][4 * gq]) * f0[u];
    st_dense(rws, lane < 48 ? WS_OP + (wg * 48 + lane) * 16 : OOB, o0, tag);
  }
  const bool isO = hw == 1 && du < 80;
  const int fc = du >> 4, fl = (du & 15) * 4 + (lane & 3);
  const int fe = (fl >> 5) * 640 + fc * 128 + (fl & 31) * 4 + (lane >> 4);
  float gsil = 0.f, xo = 0.f;
  if (isO) {
    wait_until<32>(t0 + YTIME);
    xo = lo16(__builtin_amdgcn_raw_buffer_load_b16(rx, fe * 2, 0, 0));
    const int yo = WS_YP + ((fc * 24 + (lane >> 2)) * 64 + fl) * 16;
    v4u g[2]; const int off[2] = {yo, lane < 32 ? yo + 16384 : yo};
    gather_dense<2, 16>(rws, off, tag, g);
    const f32x4 ya = dense_val(g[0]), yb = dense_val(g[1]);
    float r[4];
#pragma unroll
    for (int e = 0; e < 4; ++e) r[e] = ya[e] + (lane < 32 ? yb[e] : 0.f);
    const float y = rbf(fold4(r));
    gsil = rbf(y / (1.f + __expf(-y)));
  }
  if (du < 240) {
    const int c5 = du / 48, rg = du % 48, rb = rg * 4 + hw, hh = rb / 6;
    const int po = WS_OP + ((hh * 8 + jj) * 48 + (rb - hh * 6) * 8 + g4 * 2 + lb) * 16, po1 = po + 256 * 48 * 16;
    const rsrc_t ro = mkrs(wo, 6144 * DM * 2);
    const int vo = (rb * 32 + g4 * 8) * 1280 + c5 * 256 + col * 16, s5120 = 5120;
    lds_wait_ge<2>((volatile LAS unsigned*)&L.vtrig, 0x01010101u);
    PAD(7, 8);
    v4u wv[8], g[1], pa, pb;
    asm volatile("buffer_load_dwordx4 %0, %1, %2, 0 offen sc1" : "=v"(g[0]) : "v"(po), "s"(rws));
#pragma unroll
    for (int t = 0; t < 8; ++t)
      asm volatile("buffer_load_dwordx4 %0, %1, %2, %3 offen offset:%4 nt" : "=v"(wv[t]) : "v"(vo), "s"(ro), "s"(t < 4 ? 0 : s5120), "i"((t & 3) * 1280));
    { const v2u tv = *(volatile LAS v2u*)&L.tref;
      const unsigned due = __builtin_amdgcn_readfirstlane(tv.x + (((int)tv.y * 63) >> 8)) + TEND + TPOLL;
      wait_until<1>(due); }
    PAD(8, 9);
    asm volatile("buffer_load_dwordx4 v[232:235], %[off], %[rs], 0 offen sc1\n\ts_waitcnt vmcnt(9)"
                 : "=&{v[232:235]}"(pa), "+v"(g[0]) : [off] "v"(po1), [rs] "s"(rws) : "memory");
    if (__builtin_expect(__builtin_amdgcn_ballot_w64(dense_bad(g[0], tag) != 0u) != 0ull, 0)) {
      const int off[1] = {po};
      gather_dense<1, 4>(rws, off, tag, g);
    }
    PAD(9, 10);
    const f32x4 v0 = dense_val(g[0]);
    unsigned a0 = pkbf(sum8(v0[0]), sum8(v0[1])), a1 = pkbf(sum8(v0[2]), sum8(v0[3]));
    unsigned a2 = dppu<ROR8>(a0), a3 = dppu<ROR8>(a1);
    asm volatile("" : "+v"(a0), "+v"(a1), "+v"(a2), "+v"(a3));
    PAD(10, 11);
    v4u wT[8];
#pragma unroll
    for (int k = 0; k < 4; ++k) {
      SB();
      asm volatile("s_waitcnt vmcnt(%2)" : "+v"(wv[2 * k]), "+v"(wv[2 * k + 1]) : "i"(k < 3 ? 7 - 2 * k : 2));
      if (k == 2) asm volatile("buffer_load_dwordx4 v[236:239], %[off], %[rs], 0 offen sc1" : "=&{v[236:239]}"(pb) : [off] "v"(po1), [rs] "s"(rws) : "memory");
#pragma unroll
      for (int e = 0; e < 8; ++e) wT[e][k] = __builtin_amdgcn_perm(wv[2 * k + 1][e >> 1], wv[2 * k][e >> 1], (e & 1) ? 0x07060302u : 0x05040100u);
    }
    asm volatile("" : "+v"(wT[0]), "+v"(wT[1]), "+v"(wT[2]), "+v"(wT[3]), "+v"(wT[4]), "+v"(wT[5]), "+v"(wT[6]), "+v"(wT[7]));
    float r[4];
    PAD(11, 12);
    poll1(rws, po1, tag, r, pa, pb);
    PAD(12, 13);
    const unsigned b0 = pkbf(sum8(r[0]), sum8(r[1])), b1 = pkbf(sum8(r[2]), sum8(r[3]));
    const unsigned x0 = dppu<ROR8>(b0), x1 = dppu<ROR8>(b1);
    v4u A = {lb ? x0 : a0, lb ? x1 : a1, lb ? b0 : a2, lb ? b1 : a3};
    asm volatile("" : "+v"(A));
    PAD(13, 14);
    f32x4 dd[8];
#pragma unroll
    for (int e = 0; e < 8; ++e) dd[e] = mfma(A, wT[e], f32x4{0.f, 0.f, 0.f, 0.f});
    *(LAS f32x4*)&L.red[1][hw][lane] = (g4 & 1) ? f32x4{dd[4][0], dd[5][0], dd[6][0], dd[7][0]} : f32x4{dd[0][0], dd[1][0], dd[2][0], dd[3][0]};
    BARRIER();
    PAD(14, 15);
    if (hw == 0) st_dense(rws, WS_SP + (du * 64 + gpos) * 16, (L.red[1][0][lane] + L.red[1][1][lane]) + (L.red[1][2][lane] + L.red[1][3][lane]), tag);
    if (isO) asm volatile("s_waitcnt vmcnt(0)" ::: "memory");
    asm volatile("" ::"v"(pa), "v"(pb));
  }
  if (isO) {
    const int fo = WS_SP + ((fc * 48 + (lane >> 2)) * 64 + fl) * 16;
    PAD(15, 16);
    __builtin_amdgcn_s_sleep(SDELAY);
    v4u pa[3], pb[3]; float r[4];
    poll3(rws, fo, 16384, 32768, tag, r, pa, pb);
    PAD(18, back);
    const float res = xo + rbf(rbf(fold4(r)) * gsil);
    __builtin_amdgcn_raw_buffer_store_b16((unsigned short)pkbf(res, res), mkrs(out, 2 * DM * 2), (lane & 12) == 0 ? fe * 2 : OOB, 0, AUX_SC1);
    if (du == 0) __builtin_amdgcn_raw_buffer_store_b32(tag, rws, lane == 0 ? WS_EP : OOB, 0, AUX_SC1);
    asm volatile("" ::"v"(pa[0]), "v"(pa[1]), "v"(pa[2]), "v"(pb[0]), "v"(pb[1]), "v"(pb[2]));
  }
}

extern "C" void kernel_launch(void* const* d_in, const int* in_sizes, int n_in, void* d_out, int out_size, void* d_ws, size_t ws_size,
                              hipStream_t stream) {
  hipLaunchKernelGGL(fused_kernel, dim3(256), dim3(512), 0, stream, (const unsigned char*)d_in[0], (const unsigned char*)d_in[1],
                     (const unsigned char*)d_in[2], (const unsigned char*)d_in[3], (const unsigned char*)d_in[4], (const unsigned char*)d_in[6],
                     (const unsigned char*)d_in[7], (const unsigned char*)d_in[8], (unsigned char*)d_out, (unsigned char*)d_ws);
}
```

```cpp
#include <hip/hip_runtime.h>
#include <hip/hip_bf16.h>
#include <stdint.h>

typedef unsigned v4u __attribute__((ext_vector_type(4)));
typedef unsigned v2u __attribute__((ext_vector_type(2)));
typedef float f32x4 __attribute__((ext_vector_type(4)));
typedef float f32x2 __attribute__((ext_vector_type(2)));
typedef __bf16 bf16x2 __attribute__((ext_vector_type(2)));
typedef __bf16 bf16x8 __attribute__((ext_vector_type(8)));
#define LAS __attribute__((address_space(3)))
#define GAS __attribute__((address_space(1)))
typedef __amdgpu_buffer_rsrc_t rsrc_t;

constexpr int PFL = 6;
constexpr int DMIN = 5;
constexpr int DMAX = 12;
constexpr int GSH = 5;
constexpr int TBEG = 200;
constexpr int TEND = 2900;
constexpr int LAG = 6;
constexpr int NRND = 4;
constexpr int TS0 = 750;
constexpr int TSP = 470;
constexpr int AW1 = 90;
constexpr int AW2 = 300;
constexpr int ADELTA = 8;
constexpr int T0OFF = 30;
constexpr int QTIME = 530;
constexpr int WARM = 1500;
constexpr int YTIME = 2000;
constexpr int TPOLL = 220;
constexpr int PCAP = 300;
constexpr int MLDELAY = 68;
constexpr int SDELAY = 10;
constexpr int PSL = 6;
constexpr int FD = 5;
constexpr int ROTN = 13;

constexpr int DM = 640, SK = 4096, DH = 192, FF = 1536;
constexpr int AUX_NT = 2, AUX_SC1 = 16;
constexpr int OOB = (int)0x80000000;
constexpr int WS_EP = 0;
constexpr int WS_T0 = 128;
constexpr int WS_QX = 256;
constexpr int WS_ML = WS_QX + 72 * 3 * 64 * 16;
constexpr int WS_OP = WS_ML + 256 * 8 * 16;
constexpr int WS_SP = WS_OP + 2 * 256 * 48 * 16;
constexpr int WS_YP = WS_SP + 5 * 48 * 64 * 16;
constexpr int WS_PR = WS_YP + 5 * 24 * 64 * 16;
constexpr int WS_BC = WS_PR + NRND * 64 * 128;
constexpr int WS_END = WS_BC + NRND * 128;
constexpr unsigned SPIN_CAP = 1u << 18;
constexpr float LOG2E = 1.4426950408889634f;

__device__ __forceinline__ float asf(unsigned u) { return __builtin_bit_cast(float, u); }
__device__ __forceinline__ unsigned asu(float f) { return __builtin_bit_cast(unsigned, f); }
__device__ __forceinline__ unsigned pkbf(float lo, float hi) {
  bf16x2 v = {(__bf16)lo, (__bf16)hi};
  return __builtin_bit_cast(unsigned, v);
}
__device__ __forceinline__ float rbf(float f) { return asf(pkbf(f, f) << 16); }
__device__ __forceinline__ float lo16(unsigned u) { return asf(u << 16); }
__device__ __forceinline__ float hi16(unsigned u) { return asf(u & 0xffff0000u); }
__device__ __forceinline__ float dot2(unsigned k, unsigned q, float acc) {
  return __builtin_amdgcn_fdot2_f32_bf16(__builtin_bit_cast(bf16x2, k), __builtin_bit_cast(bf16x2, q), acc, false);
}
template <int CTRL> __device__ __forceinline__ float dpp(float x) { return asf(__builtin_amdgcn_mov_dpp(asu(x), CTRL, 0xf, 0xf, true)); }
template <int CTRL> __device__ __forceinline__ unsigned dppu(unsigned x) { return __builtin_amdgcn_mov_dpp(x, CTRL, 0xf, 0xf, true); }
constexpr int XOR1 = 0xB1, XOR2 = 0x4E, HMIR = 0x141, ROR4 = 0x124, ROR8 = 0x128;
__device__ __forceinline__ float sum8(float d) { d += dpp<XOR1>(d); d += dpp<XOR2>(d); d += dpp<HMIR>(d); return d; }
__device__ __forceinline__ float max8(float d) { d = fmaxf(d, dpp<XOR1>(d)); d = fmaxf(d, dpp<XOR2>(d)); return fmaxf(d, dpp<HMIR>(d)); }
__device__ __forceinline__ float sum_x16(float x) { auto s = __builtin_amdgcn_permlane16_swap(asu(x), asu(x), false, false); return asf(s[0]) + asf(s[1]); }
__device__ __forceinline__ float sum_x32(float x) { auto s = __builtin_amdgcn_permlane32_swap(asu(x), asu(x), false, false); return asf(s[0]) + asf(s[1]); }
__device__ __forceinline__ float max_x16(float x) { auto s = __builtin_amdgcn_permlane16_swap(asu(x), asu(x), false, false); return fmaxf(asf(s[0]), asf(s[1])); }
__device__ __forceinline__ float max_x32(float x) { auto s = __builtin_amdgcn_permlane32_swap(asu(x), asu(x), false, false); return fmaxf(asf(s[0]), asf(s[1])); }
__device__ __forceinline__ float fold4(const float (&r)[4]) {
  auto s0 = __builtin_amdgcn_permlane32_swap(asu(r[0]), asu(r[2]), false, false);
  auto s1 = __builtin_amdgcn_permlane32_swap(asu(r[1]), asu(r[3]), false, false);
  const float a = asf(s0[0]) + asf(s0[1]), b = asf(s1[0]) + asf(s1[1]);
  auto s2 = __builtin_amdgcn_permlane16_swap(asu(a), asu(b), false, false);
  float x = asf(s2[0]) + asf(s2[1]);
  x += dpp<ROR8>(x); x += dpp<ROR4>(x);
  return x;
}
__device__ __forceinline__ f32x4 mfma(v4u a, v4u b, f32x4 c) {
  return __builtin_amdgcn_mfma_f32_16x16x32_bf16(__builtin_bit_cast(bf16x8, a), __builtin_bit_cast(bf16x8, b), c, 0, 0, 0);
}
__device__ __forceinline__ v4u place(unsigned p, v4u sel) {
  return v4u{__builtin_amdgcn_perm(p, p, sel[0]), __builtin_amdgcn_perm(p, p, sel[1]), __builtin_amdgcn_perm(p, p, sel[2]), __builtin_amdgcn_perm(p, p, sel[3])};
}
__device__ __forceinline__ rsrc_t mkrs(const void* p, int bytes) { return __builtin_amdgcn_make_buffer_rsrc((void*)p, 0, bytes, 0x00020000); }
__device__ __forceinline__ unsigned clk() { return (unsigned)__builtin_amdgcn_s_memrealtime(); }

#define SB() __builtin_amdgcn_sched_barrier(0)
#define LDS_WAIT() asm volatile("s_waitcnt lgkmcnt(0)" ::: "memory")
#define BARRIER() do { LDS_WAIT(); __builtin_amdgcn_s_barrier(); asm volatile("" ::: "memory"); } while (0)
#define FLAG_SET(word, byte) (*((volatile LAS unsigned char*)&(word) + (byte)) = 1)
#define PAD(a, b) asm volatile("fk_pad" #a ":\n\ts_cbranch_execz fk_pad" #b)
#define PADS(a, b) "fk_pad" #a ":\n\ts_cbranch_execz fk_pad" #b "\n\t"

__device__ __forceinline__ void st_wide(rsrc_t rs, int off, unsigned a, unsigned b, unsigned tag) {
  __builtin_amdgcn_raw_buffer_store_b128(v4u{a, tag, b, tag}, rs, off, 0, AUX_SC1);
}
__device__ __forceinline__ void st_dense(rsrc_t rs, int off, f32x4 v, unsigned tag) {
  const unsigned ta = tag & 0x3ffu, tb = (tag >> 10) & 0x3ffu;
  __builtin_amdgcn_raw_buffer_store_b128(v4u{((asu(v[0]) + 0x200u) & ~0x3ffu) | ta, ((asu(v[1]) + 0x200u) & ~0x3ffu) | tb,
                                             ((asu(v[2]) + 0x200u) & ~0x3ffu) | ta, ((asu(v[3]) + 0x200u) & ~0x3ffu) | tb}, rs, off, 0, AUX_SC1);
}
__device__ __forceinline__ f32x4 dense_val(v4u g) { return f32x4{asf(g.x & ~0x3ffu), asf(g.y & ~0x3ffu), asf(g.z & ~0x3ffu), asf(g.w & ~0x3ffu)}; }
__device__ __forceinline__ void st_block(rsrc_t rs, int off, f32x4 v, unsigned tag) {
  const unsigned E = max(max(asu(v[0]) & 0x7fffffffu, asu(v[1]) & 0x7fffffffu), max(asu(v[2]) & 0x7fffffffu, asu(v[3]) & 0x7fffffffu)) >> 23;
  v4u g;
#pragma unroll
  for (int e = 0; e < 4; ++e) {
    const int mt = min((int)__builtin_rintf(__builtin_ldexpf(v[e], 145 - (int)E)), 0x7ffff);
    g[e] = ((unsigned)mt << 12) | (((E >> (2 * e)) & 3u) << 10) | (((e & 1) ? tag >> 10 : tag) & 0x3ffu);
  }
  __builtin_amdgcn_raw_buffer_store_b128(g, rs, off, 0, AUX_SC1);
}
__device__ __forceinline__ f32x4 block_val(v4u g) {
  const int E = ((g.x >> 10) & 3u) | ((g.y >> 8) & 12u) | ((g.z >> 6) & 48u) | ((g.w >> 4) & 192u);
  f32x4 v;
#pragma unroll
  for (int e = 0; e < 4; ++e) v[e] = E == 255 ? asf(0x7fc00000u) : __builtin_ldexpf((float)((int)g[e] >> 12), E - 145);
  return v;
}
__device__ __forceinline__ unsigned dense_bad(v4u g, unsigned tag) { return ((g.x ^ tag) | (g.z ^ tag) | (g.y ^ (tag >> 10)) | (g.w ^ (tag >> 10))) & 0x3ffu; }
template <int SLP> __device__ __forceinline__ v4u gather_wide(rsrc_t rs, int off, unsigned tag) {
  v4u g;
  unsigned spins = 0;
#pragma nounroll
  for (;;) {
    asm volatile("" ::: "memory");
    g = __builtin_amdgcn_raw_buffer_load_b128(rs, off, 0, AUX_SC1);
    if (((g.y == tag) & (g.w == tag)) || ++spins >= SPIN_CAP) break;
    __builtin_amdgcn_s_sleep(SLP);
  }
  return g;
}
template <int N, int SLP> __device__ __forceinline__ void gather_dense(rsrc_t rs, const int (&off)[N], unsigned tag, v4u (&g)[N]) {
  unsigned spins = 0;
#pragma nounroll
  for (;;) {
    asm volatile("" ::: "memory");
    unsigned bad = 0u;
#pragma unroll
    for (int k = 0; k < N; ++k) g[k] = __builtin_amdgcn_raw_buffer_load_b128(rs, off[k], 0, AUX_SC1);
#pragma unroll
    for (int k = 0; k < N; ++k) bad |= dense_bad(g[k], tag);
    if (bad == 0u || ++spins >= SPIN_CAP) break;
    __builtin_amdgcn_s_sleep(SLP);
  }
}
#define P_LD(R, SO) "buffer_load_dwordx4 " R ", %[off], %[rs], " SO " offen sc1\n\t"
#define P_BAD(T, X, Y, Z, W) "v_bitop3_b32 " T ", " X ", " Z ", %[ta] bitop3:0x7e\n\tv_bitop3_b32 %[t3], " Y ", " W ", %[tb] bitop3:0x7e\n\tv_or_b32 " T ", " T ", %[t3]\n\t"
#define P_NEXT "s_andn2_b64 %[pend], %[pend], exec\n\ts_cbranch_scc0 2f\n\ts_mov_b64 exec, %[pend]\n\t"
#define P1_TAKE(X, Y, Z, W) P_BAD("%[t0]", X, Y, Z, W) "v_and_b32 %[t0], 0x3ff, %[t0]\n\tv_cmpx_eq_u32 vcc, 0, %[t0]\n\t" \
  "v_and_b32 %[r0], %[mk], " X "\n\tv_and_b32 %[r1], %[mk], " Y "\n\tv_and_b32 %[r2], %[mk], " Z "\n\tv_and_b32 %[r3], %[mk], " W "\n\t"
__device__ __forceinline__ void poll1(rsrc_t rs, int off, unsigned tag, float (&r)[4], v4u& a, v4u& b) {
  unsigned long long pend; unsigned cnt, t0, t3;
  asm volatile(
      "s_mov_b64 %[pend], exec\n\ts_mov_b32 %[cnt], 0x40000\n"
      "1:\n\t"
      "s_waitcnt vmcnt(1)\n\t" P1_TAKE("v232", "v233", "v234", "v235") P_NEXT
      P_LD("v[232:235]", "0") "s_waitcnt vmcnt(1)\n\t" P1_TAKE("v236", "v237", "v238", "v239") P_NEXT
      P_LD("v[236:239]", "0")
      "s_sub_u32 %[cnt], %[cnt], 1\n\ts_cbranch_scc0 1b\n"
      "2:\n\ts_mov_b64 exec, -1\n\t"
      : [pend] "=&s"(pend), [cnt] "=&s"(cnt), [t0] "=&v"(t0), [t3] "=&v"(t3), [r0] "=&v"(r[0]), [r1] "=&v"(r[1]), [r2] "=&v"(r[2]), [r3] "=&v"(r[3]),
        "+{v[232:235]}"(a), "+{v[236:239]}"(b)
      : [off] "v"(off), [rs] "s"(rs), [ta] "v"(tag), [tb] "v"(tag >> 10), [mk] "s"(~0x3ffu)
      : "vcc", "scc", "memory");
}
#define P3_SUM(R, X, Y, Z) "v_and_b32 " R ", %[mk], " X "\n\tv_and_b32 %[t0], %[mk], " Y "\n\tv_and_b32 %[t1], %[mk], " Z "\n\t" \
  "v_add_f32 " R ", " R ", %[t0]\n\tv_add_f32 " R ", " R ", %[t1]\n\t"
#define P3_TAKE(A0, A1, A2, A3, B0, B1, B2, B3, C0, C1, C2, C3) \
  P_BAD("%[t0]", A0, A1, A2, A3) P_BAD("%[t1]", B0, B1, B2, B3) P_BAD("%[t2]", C0, C1, C2, C3) \
  "v_or3_b32 %[t0], %[t0], %[t1], %[t2]\n\tv_and_b32 %[t0], 0x3ff, %[t0]\n\tv_cmpx_eq_u32 vcc, 0, %[t0]\n\t" \
  P3_SUM("%[r0]", A0, B0, C0) P3_SUM("%[r1]", A1, B1, C1) P3_SUM("%[r2]", A2, B2, C2) P3_SUM("%[r3]", A3, B3, C3)
__device__ __forceinline__ void poll3(rsrc_t rs, int off, int so1, int so2, unsigned tag, float (&r)[4], v4u (&a)[3], v4u (&b)[3]) {
  unsigned long long pend; unsigned cnt, t0, t1, t2, t3;
  asm volatile(
      "s_mov_b64 %[pend], exec\n\ts_mov_b32 %[cnt], 0x40000\n\t"
      P_LD("v[208:211]", "0") P_LD("v[212:215]", "%[so1]") P_LD("v[216:219]", "%[so2]") "s_sleep %[slp]\n"
      "1:\n\t"
      P_LD("v[220:223]", "0") P_LD("v[224:227]", "%[so1]") P_LD("v[228:231]", "%[so2]") PADS(16, 17) "s_waitcnt vmcnt(3)\n\t"
      P3_TAKE("v208", "v209", "v210", "v211", "v212", "v213", "v214", "v215", "v216", "v217", "v218", "v219") P_NEXT
      P_LD("v[208:211]", "0") P_LD("v[212:215]", "%[so1]") P_LD("v[216:219]", "%[so2]") PADS(17, 18) "s_waitcnt vmcnt(3)\n\t"
      P3_TAKE("v220", "v221", "v222", "v223", "v224", "v225", "v226", "v227", "v228", "v229", "v230", "v231") P_NEXT
      "s_sub_u32 %[cnt], %[cnt], 1\n\ts_cbranch_scc0 1b\n"
      "2:\n\ts_mov_b64 exec, -1\n\ts_nop 1\n\t"
      : [pend] "=&s"(pend), [cnt] "=&s"(cnt), [t0] "=&v"(t0), [t1] "=&v"(t1), [t2] "=&v"(t2), [t3] "=&v"(t3),
        [r0] "=&v"(r[0]), [r1] "=&v"(r[1]), [r2] "=&v"(r[2]), [r3] "=&v"(r[3]),
        "=&{v[208:211]}"(a[0]), "=&{v[212:215]}"(a[1]), "=&{v[216:219]}"(a[2]), "=&{v[220:223]}"(b[0]), "=&{v[224:227]}"(b[1]), "=&{v[228:231]}"(b[2])
      : [off] "v"(off), [rs] "s"(rs), [so1] "s"(so1), [so2] "s"(so2), [ta] "v"(tag), [tb] "v"(tag >> 10), [mk] "s"(~0x3ffu), [slp] "i"(PSL)
      : "vcc", "scc", "memory");
}
template <int SLP> __device__ __forceinline__ void lds_wait_ge(volatile LAS unsigned* p, unsigned v) {
#pragma nounroll
  for (unsigned spins = 0; spins < (SPIN_CAP << 5) / SLP; ++spins) {
    if (__builtin_amdgcn_readfirstlane(*p) >= v) break;
    __builtin_amdgcn_s_sleep(SLP);
  }
  asm volatile("" ::: "memory");
}
template <int SLP> __device__ __forceinline__ void wait_until(unsigned due) {
#pragma nounroll
  for (int i = 0; i < 1536 / SLP && (int)(clk() - due) < 0; ++i) __builtin_amdgcn_s_sleep(SLP);
}

struct Lds {
  float part[2][8][DH];
  f32x4 red[2][4][64];
  unsigned xn[4][DM];
  unsigned q[2][96];
  f32x2 ml[2][4];
  unsigned short hid[4][128];
  float fac[2][4];
  unsigned qready, vtrig, gcnt, facok;
  unsigned kdone, pdone, epoch, epok;
  unsigned eo[4];
  unsigned tref, tslope;
};


__global__ void __launch_bounds__(512, 2)
fused_kernel(const unsigned char* __restrict__ x, const unsigned char* __restrict__ kc, const unsigned char* __restrict__ vc,
             const unsigned char* __restrict__ wq, const unsigned char* __restrict__ wo, const unsigned char* __restrict__ wgt,
             const unsigned char* __restrict__ wup, const unsigned char* __restrict__ wdn, unsigned char* __restrict__ out, unsigned char* ws) {
  __shared__ __attribute__((aligned(16))) Lds L;
  const int tid = threadIdx.x, lane = tid & 63, wave = __builtin_amdgcn_readfirstlane(tid >> 6);
  const int wg = blockIdx.x, h = wg >> 3, j = wg & 7;
  const int du = ((j - h) & 7) * 32 + h;
  const bool isQ = du < 216;
  const int kvo = (h * 128 + j) * 12288, rot = (ROTN * h) >> 2;
  const int voff = (wave & 3) * 3072 + (lane >> 4) * 768 + (lane & 15) * 16;
#define UNIT(r_, n_) (kvo + (r_) * (32 * 128 * 12288) + (((n_) + rot) & 15) * (8 * 12288))
  const int u0 = UNIT(0, 0), u1 = UNIT(0, 1);
  const unsigned long long t64 = __builtin_amdgcn_s_memrealtime();
  const unsigned t0 = (unsigned)t64;
  const int hw = wave & 3, g4 = lane >> 4, col = lane & 15, lb = (lane >> 3) & 1, jj = lane & 7;
  const int qt = du / 3, qr = du % 3, stride = qt < 48 ? 12288 : 3072;
  const int vq = (qr * 208 + hw * 4 + g4) * stride + (qt < 48 ? qt : qt < 60 ? qt - 48 : qt - 60) * 256 + col * 16;
  const int os = lb * 1280 + (jj + 8 * g4) * 40;
  asm volatile("" ::"s"(u0), "s"(u1), "v"(voff), "v"(vq), "v"(os));
  asm volatile("" ::"s"(x), "s"(kc), "s"(vc), "s"(wq), "s"(wo), "s"(wgt), "s"(wup), "s"(wdn), "s"(out), "s"(ws), "s"(t64));
  const rsrc_t rws = mkrs(ws, WS_END);

  const rsrc_t rk = mkrs(kc, 2 * 32 * SK * DH * 2), rv = mkrs(vc, 2 * 32 * SK * DH * 2);
  const rsrc_t rx = mkrs(x, 2 * DM * 2);
  v4u buf[64][3];
  v4u wt[14], xs0, xs1; v2u xs2;
  unsigned ep;
  if (wave >= 4) {
#pragma unroll
    for (int i = 0; i < 6; ++i) { buf[i / 3][i % 3] = __builtin_amdgcn_raw_buffer_load_b128(rk, voff + (i % 3) * 256, i < 3 ? u0 : u1, AUX_NT); SB(); }
    *(LAS v4u*)&L.qready = v4u{0u, 0u, 0u, 0u};
    *(LAS v4u*)&L.kdone = v4u{0u, 0u, 0u, 0u};
    *(LAS v4u*)&L.eo[0] = v4u{t0, t0, t0, t0};
  } else if (isQ) {
    ep = __builtin_amdgcn_raw_buffer_load_b32(rws, wave == 2 ? WS_EP : OOB, 0, AUX_SC1);
    xs0 = __builtin_amdgcn_raw_buffer_load_b128(rx, os, 0, 0); xs1 = __builtin_amdgcn_raw_buffer_load_b128(rx, os + 16, 0, 0);
    xs2 = __builtin_amdgcn_raw_buffer_load_b64(rx, os + 32, 0, 0);
    const rsrc_t rw = mkrs(qt < 48 ? wq : qt < 60 ? wgt : wup, DM * stride);
#pragma unroll
    for (int t = 0; t < 14; ++t) { wt[t] = __builtin_amdgcn_raw_buffer_load_b128(rw, (t < 13 || qr == 2) ? vq : OOB, t * 16 * stride, AUX_NT); SB(); }
    asm volatile("" ::: "memory");
    SB();
  }
  BARRIER();
  SB();
  int wave2 = wave;
  asm volatile("" : "+s"(wave2));

  if (__builtin_expect(wave2 >= 4, 1)) {
    const int w = hw;
    unsigned long long wtab;
    asm volatile("s_getpc_b64 %0\n\ts_branch 1f\n\t"
                 ".irp k,0,1,2,3,4,5,6,7,8,9,10,11,12,13,14,15\n\ts_waitcnt vmcnt(\\k)\n\ts_setpc_b64 s[98:99]\n\t.endr\n1:" : "=s"(wtab));
    wtab -= 4;
    static_assert(DMIN >= 1 && DMAX <= 16 && DMAX <= 3 * LAG, "wait table");
    unsigned ts = t0; int tb = 0;
#define ISSUE(g_) do { \
      const unsigned ev_ = now - (TBEG + (g_) * (TEND - TBEG) / 63); \
      const int lag_ = (int)(ev_ - ts) - ((g_) >= 16 ? (tb * (g_)) >> 8 : 0); \
      const unsigned long long tgt_ = wtab + 8 * min(max(PFL + (lag_ >> GSH), DMIN), DMAX); \
      const int so2_ = UNIT((g_) >> 5, (g_) & 15); \
      if ((g_) >= 6 && (g_) < 58 && ((g_) & 1) == 0) *(volatile LAS unsigned*)&L.eo[w] = ev_; \
      _Pragma("unroll") for (int i_ = 0; i_ < 3; ++i_) { \
        asm volatile("s_swappc_b64 s[98:99], %0" ::"s"(tgt_) : "s98", "s99"); SB(); \
        buf[g_][i_] = __builtin_amdgcn_raw_buffer_load_b128(((g_) & 16) ? rv : rk, voff + i_ * 256, so2_, AUX_NT); SB(); } } while (0)
#define NEXT_OF(g_) ((g_) < 2 * (16 - LAG) ? (((g_) & 1) ? -1 : 16 + (g_) / 2) : ((g_) + LAG < 64 ? (g_) + LAG : -1))
#define STEP_BEGIN() const int nx = NEXT_OF(g); const unsigned now = nx >= 0 ? clk() : 0u; \
      const v2u tsv = (nx > 16 && (nx & 1)) ? *(volatile LAS v2u*)&L.tref : v2u{0u, 0u}; SB()
#define STEP_END() do { SB(); if (nx > 16 && (nx & 1)) { ts = __builtin_amdgcn_readfirstlane(tsv.x); tb = __builtin_amdgcn_readfirstlane(tsv.y); } \
      if (nx >= 0) ISSUE(nx >= 0 ? nx : 0); } while (0)
#pragma unroll
    for (int n = 2; n < 16; ++n) { const unsigned now = clk(); SB(); ISSUE(n); }
    v4u msk;
#pragma unroll
    for (int e = 0; e < 4; ++e) msk[e] = ((jj >> 1) == e) ? ((jj & 1) ? 0xffff0000u : 0x0000ffffu) : 0u;
    lds_wait_ge<1>((volatile LAS unsigned*)&L.qready, 0x0100u);
    ts = __builtin_amdgcn_readfirstlane(*(volatile LAS unsigned*)&L.tref);
    unsigned tag = *(volatile LAS unsigned*)&L.epoch + 1u;
    int mpo = (lane & 3) == 0 ? WS_OP + ((256 + wg) * 48 + w * 16 + (lane >> 2)) * 16 : OOB;
    const LAS float* mp = (const LAS float*)&L.part[1][(lane & 3) * 2][4 * ((w * 16 + (lane >> 2)) % 48)];
#pragma unroll
    for (int r = 0; r < 2; ++r) {
      if (r == 1) lds_wait_ge<1>((volatile LAS unsigned*)&L.qready, 0x0101u);
      v4u qf[3];
#pragma unroll
      for (int i = 0; i < 3; ++i) qf[i] = *(const LAS v4u*)&L.q[r][4 * ((16 * i + col) % 24)];
      float s[16];
#pragma unroll
      for (int n = 0; n < 16; ++n) {
        const int g = 32 * r + n;
        STEP_BEGIN();
        float d0 = 0.f, d1 = 0.f, d2 = 0.f;
#pragma unroll
        for (int k = 0; k < 4; ++k) {
          d0 = dot2(buf[g][0][k], qf[0][k], d0); d1 = dot2(buf[g][1][k], qf[1][k], d1); d2 = dot2(buf[g][2][k], qf[2][k], d2);
        }
        const float dk = ((lb ? d2 : d0) + d1) + dpp<ROR8>(lb ? d0 : d2);
        s[n] = rbf(rbf(sum8(dk)) * 0.07216878364870322f);
        asm volatile("" : "+v"(s[n]));
        STEP_END();
      }
      float m = s[0];
#pragma unroll
      for (int n = 1; n < 16; ++n) m = fmaxf(m, s[n]);
      m = fmaxf(m, dpp<ROR8>(m)); m = max_x32(max_x16(m));
      float l = 0.f;
#pragma unroll
      for (int n = 0; n < 16; ++n) { const float p = __builtin_amdgcn_exp2f((s[n] - m) * LOG2E); l += p; s[n] = asf(pkbf(p, p)); }
      l += dpp<ROR8>(l); l = sum_x32(sum_x16(l));
      *(LAS f32x2*)&L.ml[r][w] = f32x2{m, l};
      if (r == 1) { LDS_WAIT(); FLAG_SET(L.kdone, w); }
      f32x4 acc[3];
#pragma unroll
      for (int i = 0; i < 3; ++i) acc[i] = f32x4{0.f, 0.f, 0.f, 0.f};
      float fac = 0.f;
#pragma unroll
      for (int n = 0; n < 16; ++n) {
        const int g = 32 * r + 16 + n;
        STEP_BEGIN();
        const unsigned pp = asu(s[n]);
        const v4u a = {pp & msk[0], pp & msk[1], pp & msk[2], pp & msk[3]};
#pragma unroll
        for (int i = 0; i < 3; ++i) acc[i] = mfma(a, buf[g][i], acc[i]);
        asm volatile("" : "+v"(acc[0]), "+v"(acc[1]), "+v"(acc[2]));
        STEP_END();
        if (nx == 63) FLAG_SET(L.vtrig, w);
        if (g == 54) PAD(0, 1);
        if (g == 57) PAD(1, 2);
        if (g == 59) PAD(2, 3);
        if (g == 61) PAD(3, 4);
        if (g == 63) PAD(4, 5);
        if (g == 60) {
          lds_wait_ge<1>((volatile LAS unsigned*)&L.facok, 1u);
          fac = *(volatile LAS float*)&L.fac[1][w];
          asm volatile("" : "+v"(mpo), "+v"(mp), "+v"(fac), "+v"(tag));
          SB();
        }
      }
      {
        LAS float* pp_ = (LAS float*)&L.part[r][w * 2 + (lane >> 5)][4 * (g4 & 1)];
        const f32x4 va = lane < 32 ? acc[0] : acc[2];
        *(LAS f32x4*)(pp_ + 8 * (col + (lane < 32 ? 0 : 8))) = r == 0 ? va : va * fac;
        if ((lane >> 5) == lb) *(LAS f32x4*)(pp_ + 8 * (jj + (lane < 32 ? 16 : 0))) = r == 0 ? acc[1] : acc[1] * fac;
      }
      if (r == 1) { asm volatile("" ::: "memory"); FLAG_SET(L.pdone, w); }
    }
    PAD(5, 6);
    if (w < 3) {
      f32x4 oa, ob;
#pragma nounroll
      for (unsigned spins = 0; spins < (SPIN_CAP << 6); ++spins) {
        const unsigned f = *(volatile LAS unsigned*)&L.pdone;
        oa = *(const volatile LAS f32x4*)mp; ob = *(const volatile LAS f32x4*)(mp + DH);
        if (__builtin_amdgcn_readfirstlane(f) == 0x01010101u) break;
      }
      PAD(6, 7);
      f32x4 o = oa + ob;
#pragma unroll
      for (int e = 0; e < 4; ++e) { o[e] += dpp<XOR1>(o[e]); o[e] += dpp<XOR2>(o[e]); }
      st_dense(rws, mpo, o, tag);
    }
    return;
  }

  int pwo = OOB;
  if (hw == 3) {
    if (lane < 12) pwo = WS_OP + ((lane / 6) * 256 + wg) * 768 + (lane % 6) * 128;
    else if (lane == 12) pwo = WS_ML + wg * 128;
    else if (lane >= 32 && lane < 40) { if (du < 240) pwo = WS_SP + du * 1024 + (lane - 32) * 128; }
    else if (lane >= 48 && lane < 56) { if (du < 120) pwo = WS_YP + du * 1024 + (lane - 48) * 128; }
  }
  const unsigned pw0 = __builtin_amdgcn_raw_buffer_load_b32(rws, pwo, 0, AUX_SC1);
  SB();
  v4u selE, selO;
#pragma unroll
  for (int e = 0; e < 4; ++e) {
    selE[e] = ((jj >> 1) == e) ? ((jj & 1) ? 0x01000c0cu : 0x0c0c0100u) : 0x0c0c0c0cu;
    selO[e] = ((jj >> 1) == e) ? ((jj & 1) ? 0x03020c0cu : 0x0c0c0302u) : 0x0c0c0c0cu;
  }
  const int gpos = (g4 >> 1) * 32 + col * 2 + (g4 & 1);
  if (isQ) {
    if (hw == 2) { *(volatile LAS unsigned*)&L.epoch = ep; *(volatile LAS unsigned*)&L.epok = 1u; }
    float xv[20];
#pragma unroll
    for (int k = 0; k < 4; ++k) { xv[2 * k] = lo16(xs0[k]); xv[2 * k + 1] = hi16(xs0[k]); xv[8 + 2 * k] = lo16(xs1[k]); xv[9 + 2 * k] = hi16(xs1[k]); }
    xv[16] = lo16(xs2[0]); xv[17] = hi16(xs2[0]); xv[18] = lo16(xs2[1]); xv[19] = hi16(xs2[1]);
    float sm = 0.f;
#pragma unroll
    for (int k = 0; k < 20; ++k) sm += xv[k];
    sm = sum_x32(sum_x16(sum8(sm)));
    const float mean = sm * (1.0f / DM);
    float sq = 0.f;
#pragma unroll
    for (int k = 0; k < 20; ++k) { const float dv = xv[k] - mean; sq = fmaf(dv, dv, sq); }
    sq = sum_x32(sum_x16(sum8(sq)));
    const float mb = rbf(mean);
    const float sd = rbf(sqrtf(rbf(rbf(sq * (1.0f / DM)) + 1e-5f)));
    const float rsd = 1.0f / sd;
    unsigned np[10];
#pragma unroll
    for (int k = 0; k < 10; ++k) {
      const unsigned dp = pkbf(xv[2 * k] - mb, xv[2 * k + 1] - mb);
      np[k] = pkbf(lo16(dp) * rsd, hi16(dp) * rsd);
    }
#pragma unroll
    for (int k = 0; k < 5; ++k) *(LAS v2u*)&L.xn[hw][lb * 320 + (jj + 8 * g4) * 10 + 2 * k] = v2u{np[2 * k], np[2 * k + 1]};
    LDS_WAIT();
    const LAS unsigned short* xp = (const LAS unsigned short*)&L.xn[hw][lb * 320] + qr * 208 + hw * 4 + g4;
    unsigned xa[14];
#pragma unroll
    for (int t = 0; t < 14; ++t) xa[t] = xp[16 * t];
    f32x4 a0 = {0.f, 0.f, 0.f, 0.f}, a1 = {0.f, 0.f, 0.f, 0.f};
#pragma unroll
    for (int t = 0; t < 14; t += 2) {
      a0 = mfma(place(xa[t], selE), wt[t], a0);
      a1 = mfma(place(xa[t + 1], selE), wt[t + 1], a1);
    }
    *(LAS f32x4*)&L.red[0][hw][lane] = a0 + a1;
    LDS_WAIT();
    FLAG_SET(L.gcnt, hw);
    if (hw == 0) {
      lds_wait_ge<1>((volatile LAS unsigned*)&L.gcnt, 0x01010101u);
      st_block(rws, WS_QX + (du * 64 + gpos) * 16, (L.red[0][0][lane] + L.red[0][1][lane]) + (L.red[0][2][lane] + L.red[0][3][lane]), *(volatile LAS unsigned*)&L.epoch + 1u);
    }
  } else if (hw == 2) {
    *(volatile LAS unsigned*)&L.epoch = __builtin_amdgcn_raw_buffer_load_b32(rws, WS_EP, 0, AUX_SC1);
    *(volatile LAS unsigned*)&L.epok = 1u;
  }
  lds_wait_ge<2>((volatile LAS unsigned*)&L.epok, 1u);
  const unsigned tag = *(volatile LAS unsigned*)&L.epoch + 1u;
  asm volatile("" ::"v"(pw0));
  if (wg == 0 && hw == 3) {
    const unsigned ta = tag & 0x3ffu, tb = (tag >> 10) & 0x3ffu;
    __builtin_amdgcn_raw_buffer_store_b128(v4u{(t0 << 10) | ta, (t0 & ~0x3ffu) | tb, ta, tb}, rws, lane < 8 ? WS_T0 + lane * 16 : OOB, 0, AUX_SC1);
  }
  wait_until<4>(t0 + QTIME);
  if (hw >= 2) {
    const int qb = hw - 2, gq = h * 48 + (lane < 48 ? lane : 0);
    const bool tl = qb == 0 && lane == 63;
    const int o0 = WS_QX + ((gq >> 5) * 192 + qb * 32 + (gq & 31)) * 16;
    v4u g[3]; const int off[3] = {tl ? WS_T0 : o0, o0 + 1024, o0 + 2048};
    gather_dense<3, 4>(rws, off, tag, g);
    if (tl) *(LAS v2u*)&L.tref = v2u{((g[0].x >> 10) | (g[0].y & ~0x3ffu)) + T0OFF, 0u};
    const f32x4 q4 = (block_val(g[0]) + block_val(g[1])) + block_val(g[2]);
    if (lane < 48) *(LAS v2u*)&L.q[qb][2 * lane] = v2u{pkbf(q4[0], q4[1]), pkbf(q4[2], q4[3])};
    LDS_WAIT();
    FLAG_SET(L.qready, 1 - qb);
  }
  if (!isQ && hw != 0) {
    const int fu = (du - 216) * 3 + hw - 1, ru = fu / 5, ct = fu % 5;
    const rsrc_t rd = mkrs(wdn, FF * DM * 2);
    const int vd = (ru * 64 + g4 * 16) * 1280 + ct * 256 + col * 16;
    v4u wd[16];
#pragma unroll
    for (int t = 0; t < 16; ++t) {
      if (t >= FD) asm volatile("s_waitcnt vmcnt(%0)" ::"i"(FD - 1));
      SB();
      wd[t] = __builtin_amdgcn_raw_buffer_load_b128(rd, vd + (t & 1) * 1280, (t >> 1) * 2560, AUX_NT);
      SB();
    }
    const int go = WS_QX + ((48 + (lane >> 5) * 12 + (ru >> 1)) * 192 + (g4 & 1) * 32 + (ru & 1) * 16 + col) * 16;
    v4u g[3]; const int off[3] = {go, go + 1024, go + 2048};
    gather_dense<3, 16>(rws, off, tag, g);
    const f32x4 gu = (block_val(g[0]) + block_val(g[1])) + block_val(g[2]);
    float hv[4];
#pragma unroll
    for (int e = 0; e < 4; ++e) { const float a = rbf(gu[e]); auto sw = __builtin_amdgcn_permlane32_swap(asu(a), asu(a), false, false); hv[e] = asf(sw[0]) * asf(sw[1]); }
    *(LAS v2u*)&L.hid[hw][4 * (lane & 31)] = v2u{pkbf(hv[0], hv[1]), pkbf(hv[2], hv[3])};
    LDS_WAIT();
    const v4u Pa = *(const volatile LAS v4u*)&L.hid[hw][lb * 64 + g4 * 16], Pb = *(const volatile LAS v4u*)&L.hid[hw][lb * 64 + g4 * 16 + 8];
    f32x4 y0 = {0.f, 0.f, 0.f, 0.f}, y1 = {0.f, 0.f, 0.f, 0.f};
#pragma unroll
    for (int k = 0; k < 8; ++k) {
      const unsigned pk = k < 4 ? Pa[k & 3] : Pb[k & 3];
      y0 = mfma(place(pk, selE), wd[2 * k], y0); y1 = mfma(place(pk, selO), wd[2 * k + 1], y1);
    }
    st_dense(rws, WS_YP + ((ct * 24 + ru) * 64 + gpos) * 16, y0 + y1, tag);
  }
  if (hw == 0) {
    lds_wait_ge<8>((volatile LAS unsigned*)&L.qready, 0x0100u);
    const unsigned tr0 = __builtin_amdgcn_readfirstlane(*(volatile LAS unsigned*)&L.tref);
    float ep_ = 0.f, gp = 0.f, bs = 0.f;
#pragma nounroll
    for (int k = 0; k < NRND; ++k) {
      const unsigned tk = tr0 + TS0 + k * TSP;
      if ((h & 3) == 0) {
        wait_until<2>(tk);
        const v4u e4 = *(volatile LAS v4u*)&L.eo[0];
        const unsigned es = (e4.x - tr0) + (e4.y - tr0) + (e4.z - tr0) + (e4.w - tr0);
        st_wide(rws, lane < 8 ? WS_PR + ((k * 64 + (h >> 2) * 8 + j) * 8 + lane) * 16 : OOB, es, es, tag);
      }
      wait_until<4>(tk + (du == 255 ? AW1 : AW2));
      v4u g;
      if (du == 255) {
        g = gather_wide<2>(rws, WS_PR + (k * 64 + lane) * 128, tag);
        float e = sum8((float)(int)g.x);
        e = sum_x32(sum_x16(e + dpp<ROR8>(e))) * (1.0f / 256);
        const float gk = ((float)(TS0 + k * TSP - TBEG) - e) * (63.0f / (TEND - TBEG)) - 1.0f;
        if (k > 0) bs = 0.5f * (bs + (e - ep_) / fmaxf(gk - gp, 1.0f));
        ep_ = e; gp = gk;
        g.x = tr0 + (int)(e - bs * gk) - ADELTA;
        g.z = (unsigned)(int)(bs * 256.0f);
        st_wide(rws, lane < 8 ? WS_BC + k * 128 + lane * 16 : OOB, g.x, g.z, tag);
      } else g = gather_wide<8>(rws, WS_BC + k * 128, tag);
      *(volatile LAS v2u*)&L.tref = v2u{g.x, g.z};
    }
  }
  if (hw == 2) {
    wait_until<16>(t0 + WARM);
    unsigned long long sv;
    asm volatile("s_mov_b64 %0, exec\n\ts_mov_b64 exec, 0\n\ts_branch fk_pad0\nfk_padback:\n\ts_mov_b64 exec, %0" : "=&s"(sv));
  }
  if (hw == 3) {
    lds_wait_ge<8>((volatile LAS unsigned*)&L.kdone, 0x01010101u);
    const f32x2 mine = *((const LAS f32x2*)&L.ml[0][0] + (lane & 7));
    st_wide(rws, lane < 8 ? WS_ML + (wg * 8 + lane) * 16 : OOB, asu(mine.x), asu(mine.y), tag);
    __builtin_amdgcn_s_sleep(MLDELAY);
    const v4u g = gather_wide<8>(rws, WS_ML + ((h * 8 + ((lane >> 2) & 7)) * 8 + (lane >> 5) * 4 + (lane & 3)) * 16, tag);
    const float m = asf(g.x);
    float M = max8(m); M = max_x16(fmaxf(M, dpp<ROR8>(M)));
    const float e = __builtin_amdgcn_exp2f((m - M) * LOG2E);
    float T = sum8(e * asf(g.z)); T = sum_x16(T + dpp<ROR8>(T));
    if (((lane >> 2) & 7) == j) L.fac[lane >> 5][lane & 3] = e / T;
    LDS_WAIT();
    *(volatile LAS unsigned*)&L.facok = 1u;
    const f32x4 f0 = *(const volatile LAS f32x4*)&L.fac[0][0];
    const int gq = lane < 48 ? lane : 0;
    f32x4 o0 = {0.f, 0.f, 0.f, 0.f};
#pragma unroll
    for (int u = 0; u < 4; ++u) o0 += (*(const LAS f32x4*)&L.part[0][2 * u][4 * gq] + *(const LAS f32x4*)&L.part[0][2 * u + 1][4 * gq]) * f0[u];
    st_dense(rws, lane < 48 ? WS_OP + (wg * 48 + lane) * 16 : OOB, o0, tag);
  }
  const bool isO = hw == 1 && du < 80;
  const int fc = du >> 4, fl = (du & 15) * 4 + (lane & 3);
  const int fe = (fl >> 5) * 640 + fc * 128 + (fl & 31) * 4 + (lane >> 4);
  float gsil = 0.f, xo = 0.f;
  if (isO) {
    wait_until<32>(t0 + YTIME);
    xo = lo16(__builtin_amdgcn_raw_buffer_load_b16(rx, fe * 2, 0, 0));
    const int yo = WS_YP + ((fc * 24 + (lane >> 2)) * 64 + fl) * 16;
    v4u g[2]; const int off[2] = {yo, lane < 32 ? yo + 16384 : yo};
    gather_dense<2, 16>(rws, off, tag, g);
    const f32x4 ya = dense_val(g[0]), yb = dense_val(g[1]);
    float r[4];
#pragma unroll
    for (int e = 0; e < 4; ++e) r[e] = ya[e] + (lane < 32 ? yb[e] : 0.f);
    const float y = rbf(fold4(r));
    gsil = rbf(y / (1.f + __expf(-y)));
  }
  if (du < 240) {
    const int c5 = du / 48, rg = du % 48, rb = rg * 4 + hw, hh = rb / 6;
    const int po = WS_OP + ((hh * 8 + jj) * 48 + (rb - hh * 6) * 8 + g4 * 2 + lb) * 16, po1 = po + 256 * 48 * 16;
    const rsrc_t ro = mkrs(wo, 6144 * DM * 2);
    const int vo = (rb * 32 + g4 * 8) * 1280 + c5 * 256 + col * 16, s5120 = 5120;
    lds_wait_ge<2>((volatile LAS unsigned*)&L.vtrig, 0x01010101u);
    PAD(7, 8);
    v4u wv[8], g[1], pa, pb;
    asm volatile("buffer_load_dwordx4 %0, %1, %2, 0 offen sc1" : "=v"(g[0]) : "v"(po), "s"(rws));
#pragma unroll
    for (int t = 0; t < 8; ++t)
      asm volatile("buffer_load_dwordx4 %0, %1, %2, %3 offen offset:%4 nt" : "=v"(wv[t]) : "v"(vo), "s"(ro), "s"(t < 4 ? 0 : s5120), "i"((t & 3) * 1280));
    { const v2u tv = *(volatile LAS v2u*)&L.tref;
      const unsigned due = __builtin_amdgcn_readfirstlane(tv.x + (((int)tv.y * 63) >> 8)) + TEND + TPOLL, cap = clk() + PCAP;
      wait_until<1>((int)(due - cap) < 0 ? due : cap); }
    PAD(8, 9);
    asm volatile("buffer_load_dwordx4 v[232:235], %[off], %[rs], 0 offen sc1\n\ts_waitcnt vmcnt(9)"
                 : "=&{v[232:235]}"(pa), "+v"(g[0]) : [off] "v"(po1), [rs] "s"(rws) : "memory");
    if (__builtin_expect(__builtin_amdgcn_ballot_w64(dense_bad(g[0], tag) != 0u) != 0ull, 0)) {
      const int off[1] = {po};
      gather_dense<1, 4>(rws, off, tag, g);
    }
    PAD(9, 10);
    const f32x4 v0 = dense_val(g[0]);
    unsigned a0 = pkbf(sum8(v0[0]), sum8(v0[1])), a1 = pkbf(sum8(v0[2]), sum8(v0[3]));
    unsigned a2 = dppu<ROR8>(a0), a3 = dppu<ROR8>(a1);
    asm volatile("" : "+v"(a0), "+v"(a1), "+v"(a2), "+v"(a3));
    PAD(10, 11);
    v4u wT[8];
#pragma unroll
    for (int k = 0; k < 4; ++k) {
      SB();
      asm volatile("s_waitcnt vmcnt(%2)" : "+v"(wv[2 * k]), "+v"(wv[2 * k + 1]) : "i"(k < 3 ? 7 - 2 * k : 2));
      if (k == 2) asm volatile("buffer_load_dwordx4 v[236:239], %[off], %[rs], 0 offen sc1" : "=&{v[236:239]}"(pb) : [off] "v"(po1), [rs] "s"(rws) : "memory");
#pragma unroll
      for (int e = 0; e < 8; ++e) wT[e][k] = __builtin_amdgcn_perm(wv[2 * k + 1][e >> 1], wv[2 * k][e >> 1], (e & 1) ? 0x07060302u : 0x05040100u);
    }
    asm volatile("" : "+v"(wT[0]), "+v"(wT[1]), "+v"(wT[2]), "+v"(wT[3]), "+v"(wT[4]), "+v"(wT[5]), "+v"(wT[6]), "+v"(wT[7]));
    float r[4];
    PAD(11, 12);
    poll1(rws, po1, tag, r, pa, pb);
    PAD(12, 13);
    const unsigned b0 = pkbf(sum8(r[0]), sum8(r[1])), b1 = pkbf(sum8(r[2]), sum8(r[3]));
    const unsigned x0 = dppu<ROR8>(b0), x1 = dppu<ROR8>(b1);
    v4u A = {lb ? x0 : a0, lb ? x1 : a1, lb ? b0 : a2, lb ? b1 : a3};
    asm volatile("" : "+v"(A));
    PAD(13, 14);
    f32x4 dd[8];
#pragma unroll
    for (int e = 0; e < 8; ++e) dd[e] = mfma(A, wT[e], f32x4{0.f, 0.f, 0.f, 0.f});
    *(LAS f32x4*)&L.red[1][hw][lane] = (g4 & 1) ? f32x4{dd[4][0], dd[5][0], dd[6][0], dd[7][0]} : f32x4{dd[0][0], dd[1][0], dd[2][0], dd[3][0]};
    BARRIER();
    PAD(14, 15);
    if (hw == 0) st_dense(rws, WS_SP + (du * 64 + gpos) * 16, (L.red[1][0][lane] + L.red[1][1][lane]) + (L.red[1][2][lane] + L.red[1][3][lane]), tag);
    if (isO) asm volatile("s_waitcnt vmcnt(0)" ::: "memory");
    asm volatile("" ::"v"(pa), "v"(pb));
  }
  if (isO) {
    const int fo = WS_SP + ((fc * 48 + (lane >> 2)) * 64 + fl) * 16;
    PAD(15, 16);
    __builtin_amdgcn_s_sleep(SDELAY);
    v4u pa[3], pb[3]; float r[4];
    poll3(rws, fo, 16384, 32768, tag, r, pa, pb);
    PAD(18, back);
    const float res = xo + rbf(rbf(fold4(r)) * gsil);
    __builtin_amdgcn_raw_buffer_store_b16((unsigned short)pkbf(res, res), mkrs(out, 2 * DM * 2), (lane & 12) == 0 ? fe * 2 : OOB, 0, AUX_SC1);
    if (du == 0) __builtin_amdgcn_raw_buffer_store_b32(tag, rws, lane == 0 ? WS_EP : OOB, 0, AUX_SC1);
    asm volatile("" ::"v"(pa[0]), "v"(pa[1]), "v"(pa[2]), "v"(pb[0]), "v"(pb[1]), "v"(pb[2]));
  }
}

extern "C" void kernel_launch(void* const* d_in, const int* in_sizes, int n_in, void* d_out, int out_size, void* d_ws, size_t ws_size,
                              hipStream_t stream) {
  hipLaunchKernelGGL(fused_kernel, dim3(256), dim3(512), 0, stream, (const unsigned char*)d_in[0], (const unsigned char*)d_in[1],
                     (const unsigned char*)d_in[2], (const unsigned char*)d_in[3], (const unsigned char*)d_in[4], (const unsigned char*)d_in[6],
                     (const unsigned char*)d_in[7], (const unsigned char*)d_in[8], (unsigned char*)d_out, (unsigned char*)d_ws);
}
```
